```python
import torch

def _softmax(x, axis=-1):
    return torch.softmax(x, dim=axis)

def _gelu(x):
    return torch.nn.functional.gelu(x, approximate='tanh')

def _silu(x):
    return torch.nn.functional.silu(x)

def _sigmoid(x):
    return torch.sigmoid(x)

def _layernorm(x, axis=-1, eps=1e-05):
    m = x.mean(dim=axis, keepdim=True)
    v = x.var(dim=axis, keepdim=True, unbiased=False)
    return (x - m) / torch.sqrt(v + eps)

def _scatter_add(tgt, idx, src, axis=0):
    return tgt.clone().index_add_(axis, idx, src)

def _segment_sum(src, seg, n):
    out = torch.zeros((n,) + src.shape[1:], dtype=src.dtype)
    return out.index_add_(0, seg, src)

def _pad(x, pw):
    flat = tuple((v for pair in reversed(pw) for v in pair))
    return torch.nn.functional.pad(x, flat)
kernel_name = 'parallel_sum__memory_aug_recurrent_mem_tr__moe_arch_mod__prompt'

def setup_inputs():

    _w = lambda a: (lambda t: t.to(torch.bfloat16) if t.is_floating_point() else t)(torch.as_tensor(a))

    def _A_setup():
        B, S, Dm = (1, 2048, 512)
        H, Dh = (32, 128)
        M, seg = (8, 32)
        seg = min(seg, S)
        S = S // seg * seg
        return {'x': _w((lambda *s: torch.randn(*s, dtype=torch.bfloat16) * 0.05)(B, S, Dm)), 'mem0': _w((lambda *s: torch.randn(*s, dtype=torch.bfloat16) * 1.6)(B, M, Dm)), 'Wq': _w((lambda *s: torch.randn(*s, dtype=torch.bfloat16) * 0.8)(Dm, H, Dh) * Dm ** (-0.5)), 'Wk': _w((lambda *s: torch.randn(*s, dtype=torch.bfloat16) * 0.8)(Dm, H, Dh) * Dm ** (-0.5)), 'Wv': _w((lambda *s: torch.randn(*s, dtype=torch.bfloat16) * 1.6)(Dm, H, Dh) * Dm ** (-0.5)), 'Wo': _w((lambda *s: torch.randn(*s, dtype=torch.bfloat16) * 1.6)(H, Dh, Dm) * (H * Dh) ** (-0.5)), 'seg_len': seg}
    _A_in = _A_setup()

    def _B_setup():
        B, S, D, F, C = (1, 2048, 512, 1792, 8)
        return {'x': _w((lambda *s: torch.randn(*s, dtype=torch.bfloat16) * 0.05)(B, S, D)), 'w_r': _w((lambda *s: torch.randn(*s, dtype=torch.bfloat16) * 0.05)(D)), 'w_gate': _w((lambda *s: torch.randn(*s, dtype=torch.bfloat16) * 0.05)(D, F)), 'w_up': _w((lambda *s: torch.randn(*s, dtype=torch.bfloat16) * 0.05)(D, F)), 'w_down': _w((lambda *s: torch.randn(*s, dtype=torch.bfloat16) * 0.05)(F, D))}
    _B_in = _B_setup()
    return {'A_x': _A_in['x'], 'A_mem0': _A_in['mem0'], 'A_Wq': _A_in['Wq'], 'A_Wk': _A_in['Wk'], 'A_Wv': _A_in['Wv'], 'A_Wo': _A_in['Wo'], 'A_seg_len': _A_in['seg_len'], 'B_x': _B_in['x'], 'B_w_r': _B_in['w_r'], 'B_w_gate': _B_in['w_gate'], 'B_w_up': _B_in['w_up'], 'B_w_down': _B_in['w_down']}

def reference(A_x, A_mem0, A_Wq, A_Wk, A_Wv, A_Wo, A_seg_len, B_x, B_w_r, B_w_gate, B_w_up, B_w_down):

    def _A_ref(x, mem0, Wq, Wk, Wv, Wo, seg_len):
        B, S, Dm = x.shape
        H, Dh = (Wq.shape[1], Wq.shape[2])
        M = mem0.shape[1]
        scale = Dh ** (-0.5)
        n_seg = S // seg_len
        L = M + seg_len
        idx = torch.arange(L)
        mask = (idx[:, None] < M) | (idx[:, None] >= idx[None, :])
        mem = mem0
        outs = []
        for s in range(n_seg):
            xs = x[:, s * seg_len:(s + 1) * seg_len, :].clone()
            h = torch.concatenate([mem, xs], axis=1)
            q = torch.einsum('btd,dhe->bhte', h, Wq)
            k = torch.einsum('btd,dhe->bhte', h, Wk)
            v = torch.einsum('btd,dhe->bhte', h, Wv)
            scores = torch.einsum('bhqe,bhke->bhqk', q, k) * scale
            scores = torch.where(mask[None, None], scores, -1000000000.0)
            p = _softmax(scores, axis=-1)
            o = torch.einsum('bhqk,bhke->bhqe', p, v)
            o = torch.einsum('bhte,hed->btd', o, Wo)
            mem = o[:, :M, :].clone()
            outs.append(o[:, M:, :])
        return torch.concatenate(outs, axis=1)

    def _B_ref(x, w_r, w_gate, w_up, w_down):
        B, S, D = x.shape
        C = 8
        r = x @ w_r
        idx = torch.argsort(-r, axis=-1)[:, :C]
        idx_e = (lambda x, n, axis: torch.repeat_interleave(x, n, dim=axis))(idx.reshape(B, C, 1), D, axis=2)
        x_sel = (lambda x, i, axis: torch.take_along_dim(x, i, dim=axis))(x, idx_e, axis=1)
        g = _silu(x_sel @ w_gate)
        u = x_sel @ w_up
        y = g * u @ w_down
        r_sel = _sigmoid((lambda x, i, axis: torch.take_along_dim(x, i, dim=axis))(r, idx, axis=1))
        y = y * r_sel.reshape(B, C, 1)
        b_off = (torch.arange(B) * S).reshape(B, 1)
        flat_idx = (idx + b_off).reshape(B * C)
        zero = (x * 0.0).reshape(B * S, D)
        upd = _scatter_add(zero, flat_idx, y.reshape(B * C, D), axis=0)
        return x + upd.reshape(B, S, D)
    x = A_x
    xn = _layernorm(x, axis=-1)
    return x + _A_ref(xn, A_mem0, A_Wq, A_Wk, A_Wv, A_Wo, A_seg_len) + _B_ref(xn, B_w_r, B_w_gate, B_w_up, B_w_down)
```

```cpp
#include <hip/hip_runtime.h>
#include <hip/hip_bf16.h>
#include <stdint.h>
#include <type_traits>

typedef unsigned short u16;
typedef unsigned int u32;
typedef unsigned long long u64;
typedef __bf16 bf16x2 __attribute__((ext_vector_type(2)));
typedef __bf16 bf16x8 __attribute__((ext_vector_type(8)));
typedef __bf16 bf16x16 __attribute__((ext_vector_type(16)));
typedef short s16x4 __attribute__((ext_vector_type(4)));
typedef float f32x2 __attribute__((ext_vector_type(2)));
typedef float f32x4 __attribute__((ext_vector_type(4)));
typedef u32 u32x2 __attribute__((ext_vector_type(2)));
typedef u32 u32x4 __attribute__((ext_vector_type(4)));
typedef u32 u32x8 __attribute__((ext_vector_type(8)));
#define LAS __attribute__((address_space(3)))
#define GAS __attribute__((address_space(1)))
typedef LAS unsigned char* ldsp;
typedef __amdgpu_buffer_rsrc_t rsrc_t;
#define DEV __device__ __forceinline__
#define RLX_AGENT __ATOMIC_RELAXED, __HIP_MEMORY_SCOPE_AGENT
#define SC1 16

constexpr int NSEG = 64, DM = 512, NH = 32, FF = 1792;
constexpr u32 SENT = 0xFFFFFFFFu;
constexpr size_t CTRL_BYTES = 49152;
constexpr int L_TICKET = 0, L_OVF = 8, L_ABORT = 9, L_KV = 16, L_O = 80, L_M1 = 112, L_M2 = 144, L_M3 = 176, L_TBL = 208, L_CH = 216;
constexpr size_t OFF_XCH = 1u << 20;
constexpr size_t OFF_KX = 8u << 20;
constexpr size_t OFF_VX = 24u << 20;
constexpr size_t OFF_O = 40u << 20;
constexpr size_t OFF_KM = 56u << 20;
constexpr size_t OFF_PM = 60u << 20;
constexpr size_t OFF_MOE = 64u << 20;
constexpr size_t OFF_XN = 68u << 20;
constexpr size_t OFF_DBG = 72u << 20;
constexpr size_t OFF_VWM = 80u << 20;
constexpr size_t OFF_VW = 96u << 20;
constexpr u32 MOE_R = 0, MOE_H = 4096, MOE_U = 4096 + 32768, MOE_I = 4096 + 32768 + 8192;
constexpr int LDS_BYTES = 160 * 1024;

#define AUX1 16
#define AUX2 16
#define DELTA2 14
#define PD1MIN 3
#define PD1 14
#define PD2 7

struct Params {
  const u16* x; const u16* mem0; const u16* wq; const u16* wk; const u16* wv; const u16* wo;
  const u16* wr; const u16* wg; const u16* wu; const u16* wd;
  u16* out; unsigned char* ws;
};

DEV float bflo(u32 u) { return __uint_as_float(u << 16); }
DEV float bfhi(u32 u) { return __uint_as_float(u & 0xffff0000u); }
DEV u32 pk2(float a, float b) { f32x2 f = {a, b}; return __builtin_bit_cast(u32, __builtin_convertvector(f, bf16x2)); }
DEV float rbf(float a) { return bflo(pk2(a, a)); }
DEV u32 nosent(u32 v) { return v < 0xFFFFFFFEu ? v : 0xFFFFFFFEu; }
template <int N> DEV float ror16(float v) { return __int_as_float(__builtin_amdgcn_update_dpp(0, __float_as_int(v), 0x120 + N, 0xf, 0xf, false)); }
DEV float row16_sum(float v) { v += ror16<8>(v); v += ror16<4>(v); v += ror16<2>(v); v += ror16<1>(v); return v; }
DEV void swap32(float& a, float& b) { auto r = __builtin_amdgcn_permlane32_swap(__float_as_uint(a), __float_as_uint(b), false, false); a = __uint_as_float(r[0]); b = __uint_as_float(r[1]); }
DEV void swap16(float& a, float& b) { auto r = __builtin_amdgcn_permlane16_swap(__float_as_uint(a), __float_as_uint(b), false, false); a = __uint_as_float(r[0]); b = __uint_as_float(r[1]); }
DEV float xmax_g(float v) { float a = v, b = v; swap16(a, b); v = fmaxf(a, b); a = v; b = v; swap32(a, b); return fmaxf(a, b); }
DEV float xsum_g(float v) { float a = v, b = v; swap16(a, b); v = a + b; a = v; b = v; swap32(a, b); return a + b; }
DEV s16x4 trread(ldsp p) { return __builtin_amdgcn_ds_read_tr16_b64_v4i16((LAS s16x4*)p); }
DEV u32x2 tr2(ldsp p) { return __builtin_bit_cast(u32x2, trread(p)); }
DEV u32x4 lds16(ldsp p) { return *(LAS u32x4*)p; }
DEV u32x2 lds8(ldsp p) { return *(LAS u32x2*)p; }
DEV f32x4 mfma32(u32x4 a, u32x4 b, f32x4 c) { return __builtin_amdgcn_mfma_f32_16x16x32_bf16(__builtin_bit_cast(bf16x8, a), __builtin_bit_cast(bf16x8, b), c, 0, 0, 0); }
DEV f32x4 mfma16(u32x2 a, u32x2 b, f32x4 c) { return __builtin_amdgcn_mfma_f32_16x16x16bf16_1k(__builtin_bit_cast(s16x4, a), __builtin_bit_cast(s16x4, b), c, 0, 0, 0); }
DEV f32x4 smf(u32x4 a, u32x8 b, f32x4 c, int idx) { return __builtin_amdgcn_smfmac_f32_16x16x64_bf16(__builtin_bit_cast(bf16x8, a), __builtin_bit_cast(bf16x16, b), c, idx, 0, 0); }
DEV rsrc_t mkrsrc(const void* p, u32 bytes) { return __builtin_amdgcn_make_buffer_rsrc((void*)p, 0, bytes, 0x00020000); }
DEV u32x4 ld16s(rsrc_t r, u32 off) { return __builtin_amdgcn_raw_buffer_load_b128(r, off, 0, SC1); }
DEV u32x2 ld8s(rsrc_t r, u32 off) { return __builtin_amdgcn_raw_buffer_load_b64(r, off, 0, SC1); }
DEV u32 ld4s(rsrc_t r, u32 off) { return __builtin_amdgcn_raw_buffer_load_b32(r, off, 0, SC1); }
DEV void st16o(u32x4 v, rsrc_t r, u32 off, u32 so) { __builtin_amdgcn_raw_buffer_store_b128(v, r, off, so, SC1); asm volatile("s_nop 1" :: "v"(v)); }
DEV void st16s(rsrc_t r, u32 off, u32x4 v) { st16o(v, r, off, 0); }
DEV u32x4 ld16o(rsrc_t r, u32 off, u32 so) { return __builtin_amdgcn_raw_buffer_load_b128(r, off, so, SC1); }
DEV void st8s(rsrc_t r, u32 off, u32x2 v) { __builtin_amdgcn_raw_buffer_store_b64(v, r, off, 0, SC1); }
DEV void st4s(rsrc_t r, u32 off, u32 v) { __builtin_amdgcn_raw_buffer_store_b32(v, r, off, 0, SC1); }
DEV void drain() { asm volatile("s_waitcnt vmcnt(0)" ::: "memory"); }
DEV void cbar() { asm volatile("" ::: "memory"); }
DEV void bar() { __syncthreads(); }
DEV int uni(int v) { return __builtin_amdgcn_readfirstlane(v); }

struct Ctl {
  GAS u32* c;
  bool dead;
  DEV GAS u32* line(int l) const { return c + l * 32; }
  DEV bool spin(u32& n) {
    if (++n > (1u << 17)) { __hip_atomic_store(line(L_ABORT), 1u, RLX_AGENT); dead = true; return true; }
    if ((n & 255u) == 0u && uni(__hip_atomic_load(line(L_ABORT), RLX_AGENT))) { dead = true; return true; }
    return false;
  }
  DEV u32 wait_ge(int l, u32 want) {
    u32 n = 0, v = want;
    if (dead) return want;
    for (;;) {
      v = uni(__hip_atomic_load(line(l), RLX_AGENT));
      if (v >= want) break;
      __builtin_amdgcn_s_sleep(4);
      if (spin(n)) { v = want; break; }
    }
    __builtin_amdgcn_fence(__ATOMIC_ACQUIRE, "wavefront");
    return v;
  }
  DEV void wait_lanes(int l, u32 want, u32& seen) {
    u32 n = 0;
    if (!__all(seen >= want) && !dead) {
      for (;;) {
        seen = __hip_atomic_load(line(l), RLX_AGENT);
        if (__all(seen >= want)) break;
        __builtin_amdgcn_s_sleep(2);
        if (spin(n)) break;
      }
    }
    __builtin_amdgcn_fence(__ATOMIC_ACQUIRE, "wavefront");
  }
  DEV void post(int l, u32 v) { __hip_atomic_store(line(l), v, RLX_AGENT); }
};

#define RTA(i) do { } while (0)
#define RT(ev) do { } while (0)
#define RTS(s) do { } while (0)
struct LnRow { float mean, rinv; };
DEV void ln_load(const u16* xrow, int c16, u32x4 (&v)[4]) {
#pragma unroll
  for (int i = 0; i < 4; ++i) v[i] = *(const u32x4*)(xrow + (c16 + 16 * i) * 8);
}
DEV LnRow ln_stats(const u32x4 (&v)[4]) {
  float s = 0.f;
#pragma unroll
  for (int i = 0; i < 4; ++i)
#pragma unroll
    for (int j = 0; j < 4; ++j) s += bflo(v[i][j]) + bfhi(v[i][j]);
  const float mean = row16_sum(s) * (1.f / 512.f);
  float q = 0.f;
#pragma unroll
  for (int i = 0; i < 4; ++i)
#pragma unroll
    for (int j = 0; j < 4; ++j) { float a = bflo(v[i][j]) - mean, b = bfhi(v[i][j]) - mean; q += a * a + b * b; }
  const float var = row16_sum(q) * (1.f / 512.f);
  const float sd = rbf(__builtin_sqrtf(rbf(rbf(var) + 1e-5f)));
  LnRow r; r.mean = rbf(mean); r.rinv = 1.0f / sd;
  return r;
}
DEV u32 ln_pair(u32 u, LnRow r) { u32 d = pk2(bflo(u) - r.mean, bfhi(u) - r.mean); return pk2(bflo(d) * r.rinv, bfhi(d) * r.rinv); }
DEV u32x4 ln_apply(u32x4 v, LnRow r) { u32x4 o; o[0] = ln_pair(v[0], r); o[1] = ln_pair(v[1], r); o[2] = ln_pair(v[2], r); o[3] = ln_pair(v[3], r); return o; }
DEV u32 xn_off(int row, int chunk) { return row * 1024 + ((chunk ^ (row & 15)) << 4); }

template <int MODE> DEV int wswz(int row) { return MODE == 0 ? ((row & 3) | (((row >> 3) & 1) << 2)) : ((row & 1) | (((row >> 2) & 3) << 1)); }
DEV void wraw_load(const u16* w, int h, int tid, u32x4 (&r)[16]) {
  const rsrc_t rs = mkrsrc(w, 4u << 20);
  const u32 vo = (u32)(tid >> 4) * 8192 + (tid & 15) * 16;
#pragma unroll
  for (int i = 0; i < 16; ++i) r[i] = __builtin_amdgcn_raw_buffer_load_b128(rs, vo, h * 256 + i * 262144, 0);
}
template <int MODE> DEV void wraw_store(ldsp L, int tid, const u32x4 (&r)[16]) {
#pragma unroll
  for (int i = 0; i < 16; ++i) { const int row = i * 32 + (tid >> 4), c = tid & 15; *(LAS u32x4*)(L + row * 256 + (((c >> 1) ^ wswz<MODE>(row)) << 5) + (c & 1) * 16) = r[i]; }
}
DEV u32x4 wfrag_dense(ldsp L, int lane, int et, int ks) {
  const int g = lane >> 4, q = (lane >> 2) & 3, pp = lane & 3;
  const int r0 = 32 * ks + 8 * g + q;
  const u32 a = r0 * 256 + ((et ^ wswz<0>(r0)) << 5) + pp * 8;
  u32x2 lo = tr2(L + a), hi = tr2(L + a + 1024);
  return (u32x4){lo[0], lo[1], hi[0], hi[1]};
}

DEV void xn_fetch(Ctl& C, const unsigned char* xn, int s, int tid, ldsp dst, u32& seen) {
  C.wait_lanes(L_M1 + (tid & 31), (tid & 31) == (s >> 1) ? 1u : 0u, seen);
  const int w = uni(tid >> 6);
#pragma unroll
  for (int i = 0; i < 4; ++i)
    __builtin_amdgcn_global_load_lds((const GAS u32*)(xn + (size_t)s * 32768 + i * 8192 + tid * 16), (LAS u32*)(dst + i * 8192 + w * 1024), 16, 0, SC1);
}
DEV u32x4 proj_kfmt(ldsp XN, const u32x4 (&wf)[32], int lane, int rt) {
  f32x4 a0 = {0, 0, 0, 0}, a1 = {0, 0, 0, 0};
  asm volatile("" : "+v"(lane));
  const int row = 16 * rt + (lane & 15), g = lane >> 4;
  ldsp b[4];
#pragma unroll
  for (int k = 0; k < 4; ++k) b[k] = XN + xn_off(row, 4 * k + g);
  u32x4 xf[4];
#pragma unroll
  for (int k = 0; k < 4; ++k) xf[k] = lds16(b[k]);
#pragma unroll
  for (int ks = 0; ks < 16; ++ks) {
    __builtin_amdgcn_sched_barrier(0);
    a0 = mfma32(wf[ks], xf[ks & 3], a0); a1 = mfma32(wf[16 + ks], xf[ks & 3], a1);
    __builtin_amdgcn_sched_barrier(0);
    if (ks + 4 < 16) xf[ks & 3] = lds16(b[ks & 3] + ((ks >> 2) + 1) * 256);
  }
  return (u32x4){pk2(a0[0], a0[1]), pk2(a0[2], a0[3]), pk2(a1[0], a1[1]), pk2(a1[2], a1[3])};
}

DEV void role_k(const Params& P, Ctl& C, ldsp L, int h) {
  const int tid = threadIdx.x, lane = tid & 63, w = uni(tid >> 6);
  u32x4 wf[32];
  {
    u32x4 r[16];
    wraw_load(P.wk, h, tid, r);
    wraw_store<0>(L, tid, r);
    bar();
#pragma unroll
    for (int ks = 0; ks < 16; ++ks) { wf[ks] = wfrag_dense(L, lane, 2 * (w >> 1), ks); wf[16 + ks] = wfrag_dense(L, lane, 2 * (w >> 1) + 1, ks); }
    bar();
  }
  RT(1);
  const rsrc_t dst = mkrsrc(P.ws + OFF_KX, 16u << 20);
  u32 seenX = 0;
  xn_fetch(C, P.ws + OFF_XN, 0, tid, L, seenX);
  for (int s = 0; s < NSEG; ++s) {
    ldsp XN = L + (s & 1) * 32768;
    drain();
    bar();
    if (tid == 0 && s > 0) C.post(L_KV + h, s);
    if (s + 1 < NSEG) xn_fetch(C, P.ws + OFF_XN, s + 1, tid, L + ((s + 1) & 1) * 32768, seenX);
    const u32x4 o = proj_kfmt(XN, wf, lane, w & 1);
    st16s(dst, (u32)(s * 32 + h) * 8192 + ((w & 1) * 4 + (w >> 1)) * 1024 + lane * 16, o);
    RTS(s);
  }
  drain();
  bar();
  if (tid == 0) C.post(L_KV + h, NSEG);
}

DEV void role_v(const Params& P, Ctl& C, ldsp L, int h) {
  const int tid = threadIdx.x, lane = tid & 63, w = uni(tid >> 6), g = lane >> 4;
  u32x4 wf[32], wo[16];
  {
    u32x4 r[16];
    wraw_load(P.wv, h, tid, r);
    wraw_store<0>(L, tid, r);
    const rsrc_t rs = mkrsrc(P.wo, 4u << 20);
#pragma unroll
    for (int i = 0; i < 16; ++i) r[i] = __builtin_amdgcn_raw_buffer_load_b128(rs, (u32)(tid >> 4) * 1024 + (tid & 15) * 16, h * 131072 + (i & 3) * 32768 + (i >> 2) * 256, 0);
    bar();
#pragma unroll
    for (int ks = 0; ks < 16; ++ks) { wf[ks] = wfrag_dense(L, lane, 2 * (w >> 1), ks); wf[16 + ks] = wfrag_dense(L, lane, 2 * (w >> 1) + 1, ks); }
    bar();
    wraw_store<0>(L, tid, r);
    bar();
#pragma unroll
    for (int i = 0; i < 16; ++i) {
      const int et = (4 * w + (i >> 2)) & 7, r0 = (w >> 1) * 128 + (i & 3) * 32 + 4 * g + ((lane >> 2) & 3), r1 = r0 + 16;
      const u32x2 lo = tr2(L + r0 * 256 + ((et ^ wswz<0>(r0)) << 5) + (lane & 3) * 8), hi = tr2(L + r1 * 256 + ((et ^ wswz<0>(r1)) << 5) + (lane & 3) * 8);
      wo[i] = (u32x4){lo[0], lo[1], hi[0], hi[1]};
    }
    bar();
  }
  RT(1);
  const rsrc_t dv = mkrsrc(P.ws + OFF_VX, 16u << 20), dw = mkrsrc(P.ws + OFF_VW, 64u << 20);
  ldsp VT = L + 65536;
  const u32 a_vt = (u32)(w >> 1) * 1024 + ((lane & 3) * 16 + 4 * g + ((lane >> 2) & 3)) * 16 + (w & 1) * 8;
  u32 seenX = 0;
  xn_fetch(C, P.ws + OFF_XN, 0, tid, L, seenX);
  drain();
  bar();
  for (int s = 0; s < NSEG; ++s) {
    ldsp XN = L + (s & 1) * 32768;
    if (s + 1 < NSEG) xn_fetch(C, P.ws + OFF_XN, s + 1, tid, L + ((s + 1) & 1) * 32768, seenX);
    const u32x4 vk = proj_kfmt(XN, wf, lane, w & 1);
    VT = L + 65536 + (s & 1) * 8192;
    *(LAS u32x4*)(VT + ((w & 1) * 4 + (w >> 1)) * 1024 + lane * 16) = vk;
    drain();
    bar();
    if (tid == 0 && s > 2) C.post(L_KV + 32 + h, s);
    {
      const u32x2 lo = tr2(VT + a_vt), hi = tr2(VT + 4096 + a_vt);
      st16s(dv, (u32)(s * 32 + h) * 8192 + w * 1024 + lane * 16, (u32x4){lo[0], lo[1], hi[0], hi[1]});
    }
#pragma unroll
    for (int hf = 0; hf < 2; ++hf) {
      f32x4 c[4];
#pragma unroll
      for (int i = 0; i < 4; ++i) c[i] = (f32x4){0, 0, 0, 0};
#pragma unroll
      for (int ks = 0; ks < 4; ++ks) {
        const u32x4 f0 = lds16(VT + ks * 1024 + lane * 16), f1 = lds16(VT + 4096 + ks * 1024 + lane * 16);
#pragma unroll
        for (int c2 = 0; c2 < 2; ++c2) { c[2 * c2] = mfma32(f0, wo[(hf * 2 + c2) * 4 + ks], c[2 * c2]); c[2 * c2 + 1] = mfma32(f1, wo[(hf * 2 + c2) * 4 + ks], c[2 * c2 + 1]); }
      }
#pragma unroll
      for (int c2 = 0; c2 < 2; ++c2)
        st16s(dw, (u32)(s * 32 + h) * 32768 + (4 * w + hf * 2 + c2) * 1024 + lane * 16,
              (u32x4){pk2(c[2 * c2][0], c[2 * c2][1]), pk2(c[2 * c2][2], c[2 * c2][3]), pk2(c[2 * c2 + 1][0], c[2 * c2 + 1][1]), pk2(c[2 * c2 + 1][2], c[2 * c2 + 1][3])});
      __builtin_amdgcn_sched_barrier(0);
    }
    if (s < 2) {
      drain();
      bar();
      if (tid == 0) C.post(L_KV + 32 + h, s + 1);
    }
    RTS(s);
  }
  drain();
  bar();
  if (tid == 0) C.post(L_KV + 32 + h, NSEG);
}

DEV void role_attn(const Params& P, Ctl& C, ldsp L, unsigned char* xch, int h, bool far) {
  const int tid = threadIdx.x, lane = tid & 63, w = uni(tid >> 6);
  if (w < 6) {
    const int pi = h * 6 + w;
    const rsrc_t rx = mkrsrc(xch + (size_t)pi * 16384, 16384);
    u32 b0 = ~0u, b1 = ~0u;
#pragma unroll
    for (int it = 0; it < 8; ++it) {
      const u32 off = (it & 1) * 8192 + (it >> 1) * 1024 + (lane & 31) * 4;
      cbar();
      const u64 t0 = __builtin_amdgcn_s_memtime();
      __builtin_amdgcn_s_waitcnt(0xC07F);
      st4s(rx, off, SENT);
      drain();
      const u64 t1 = __builtin_amdgcn_s_memtime();
      __builtin_amdgcn_s_waitcnt(0xC07F);
      const u32 dt = (u32)(t1 - t0);
      if (it & 1) b1 = dt < b1 ? dt : b1; else b0 = dt < b0 ? dt : b0;
    }
    const u32 sel = uni(((b1 < b0) != far) ? 1 : 0);
    const u32x4 z = {SENT, SENT, SENT, SENT};
#pragma unroll
    for (int i = 0; i < 8; ++i) st16s(rx, sel * 8192 + i * 1024 + lane * 16, z);
    drain();
    if (lane == 0) __hip_atomic_store(C.line(L_TBL) + pi, sel + 1u, RLX_AGENT);
  }
  RT(2);
  u32x4 wf[32];
  {
    u32x4 r[16];
    wraw_load(P.wq, h, tid, r);
    wraw_store<0>(L, tid, r);
    bar();
#pragma unroll
    for (int ks = 0; ks < 16; ++ks) { wf[ks] = wfrag_dense(L, lane, 2 * (w >> 1), ks); wf[16 + ks] = wfrag_dense(L, lane, 2 * (w >> 1) + 1, ks); }
    bar();
  }
  const rsrc_t rk = mkrsrc(P.ws + OFF_KX, 16u << 20), rv = mkrsrc(P.ws + OFF_VX, 16u << 20), ro = mkrsrc(P.ws + OFF_O, 16u << 20),
               rm = mkrsrc(P.ws + OFF_KM, 4u << 20), rp = mkrsrc(P.ws + OFF_PM, 1u << 20);
  RT(1);
  ldsp QL = L + 65536, KL = L + 65536 + 8192;
  const int rt = w & 1, q4 = w >> 1, g = lane >> 4, tl = lane & 15;
  u32 seenKV = 0, seenM = 0;
  u32 seenX = 0;
  xn_fetch(C, P.ws + OFF_XN, 0, tid, L, seenX);
  drain();
  bar();
  for (int s = 0; s < NSEG; ++s) {
    ldsp XN = L + (s & 1) * 32768;
    QL = L + 65536 + (s & 1) * 16384; KL = QL + 8192;
    const u32 base = (u32)(s * 32 + h) * 8192;
    RTA(0);
    C.wait_lanes(L_KV + (lane & 1) * 32 + h, s + 1, seenKV);
    const u32x4 kx = ld16s(rk, base + tid * 16);
    const u32x4 vx0 = ld16s(rv, base + (2 * q4) * 1024 + lane * 16), vx1 = ld16s(rv, base + (2 * q4 + 1) * 1024 + lane * 16);
    const bool early = seenM >= (u32)s + 1;
    u32x4 km[4];
    u32 fm = 0;
    if (early) {
#pragma unroll
      for (int p = 0; p < 4; ++p) km[p] = ld16s(rm, (u32)(s * 32 + h) * 2048 + p * 512 + g * 128 + (tl & 7) * 16);
    }
    if (seenM < (u32)s + 3) fm = __hip_atomic_load(C.line(L_CH + 4 * h), RLX_AGENT);
    if (s + 1 < NSEG) xn_fetch(C, P.ws + OFF_XN, s + 1, tid, L + ((s + 1) & 1) * 32768, seenX);
    RTA(2);
    const u32x4 qf = proj_kfmt(XN, wf, lane, rt);
    *(LAS u32x4*)(QL + (rt * 4 + q4) * 1024 + lane * 16) = qf;
    *(LAS u32x4*)(KL + tid * 16) = kx;
    drain();
    RTA(3);
    bar();
    RTA(4);
    if (tid == 0 && s > 0 && s <= NSEG - 6) C.post(L_O + h, s);
    { const u32 f = uni(fm); seenM = f > seenM ? f : seenM; }
    f32x4 sc[3];
    u32x4 qq[4];
#pragma unroll
    for (int kt = 0; kt < 3; ++kt) sc[kt] = (f32x4){0, 0, 0, 0};
#pragma unroll
    for (int p = 0; p < 4; ++p) {
      qq[p] = lds16(QL + (rt * 4 + p) * 1024 + lane * 16);
      sc[1] = mfma32(lds16(KL + p * 1024 + lane * 16), qq[p], sc[1]);
      sc[2] = mfma32(lds16(KL + (4 + p) * 1024 + lane * 16), qq[p], sc[2]);
    }
    RTA(5);
    if (!early) {
      if (seenM < (u32)s + 1) seenM = C.wait_ge(L_CH + 4 * h, s + 1);
      __builtin_amdgcn_fence(__ATOMIC_ACQUIRE, "wavefront");
#pragma unroll
      for (int p = 0; p < 4; ++p) km[p] = ld16s(rm, (u32)(s * 32 + h) * 2048 + p * 512 + g * 128 + (tl & 7) * 16);
    }
#pragma unroll
    for (int p = 0; p < 4; ++p) sc[0] = mfma32(km[p], qq[p], sc[0]);
    RTA(6);
    const float cs = 0.08838834764831845f * 1.4426950408889634f;
    const int tq = 16 * rt + tl;
    float mx = -3.0e38f;
#pragma unroll
    for (int kt = 0; kt < 3; ++kt)
#pragma unroll
      for (int r = 0; r < 4; ++r) {
        const bool ok = kt == 0 ? (g < 2) : (16 * (kt - 1) + 4 * g + r <= tq);
        const float v = ok ? sc[kt][r] : -__builtin_inff();
        sc[kt][r] = v; mx = fmaxf(mx, v);
      }
    mx = xmax_g(mx) * cs;
    float sum = 0.f;
#pragma unroll
    for (int kt = 0; kt < 3; ++kt)
#pragma unroll
      for (int r = 0; r < 4; ++r) { const float e = __builtin_amdgcn_exp2f(__builtin_fmaf(sc[kt][r], cs, -mx)); sc[kt][r] = e; sum += e; }
    sum = xsum_g(sum);
    const float inv = __builtin_amdgcn_rcpf(sum);
    const u32x2 pm = {pk2(sc[0][0] * inv, sc[0][1] * inv), pk2(sc[0][2] * inv, sc[0][3] * inv)};
    const u32x4 px = {pk2(sc[1][0] * inv, sc[1][1] * inv), pk2(sc[1][2] * inv, sc[1][3] * inv), pk2(sc[2][0] * inv, sc[2][1] * inv), pk2(sc[2][2] * inv, sc[2][3] * inv)};
    if (q4 == 0 && g < 2) st8s(rp, (u32)(s * 32 + h) * 512 + tq * 16 + g * 8, pm);
    const f32x4 z = {0, 0, 0, 0};
    const f32x4 o0 = mfma32(vx0, px, z), o1 = mfma32(vx1, px, z);
    const u32x4 of = {pk2(o0[0], o0[1]), pk2(o0[2], o0[3]), pk2(o1[0], o1[1]), pk2(o1[2], o1[3])};
    st16s(ro, base + (rt * 4 + q4) * 1024 + lane * 16, of);
    RTA(7);
    if (s >= NSEG - 6) {
      drain();
      bar();
      if (tid == 0) C.post(L_O + h, s + 1);
    }
    RTS(s);
  }
}

DEV void role_out(const Params& P, Ctl& C, ldsp L, int ct, int par) {
  const int tid = threadIdx.x, lane = tid & 63, w = uni(tid >> 6), g = lane >> 4, tl = lane & 15;
  u32x4 wf[32];
  bar();
#pragma unroll
  for (int c2 = 0; c2 < 2; ++c2) {
    u32x4 r[16];
    const rsrc_t rs = mkrsrc(P.wo, 4u << 20);
#pragma unroll
    for (int i = 0; i < 16; ++i) r[i] = __builtin_amdgcn_raw_buffer_load_b128(rs, (u32)(tid >> 1) * 1024 + (tid & 1) * 16, i * 262144 + ct * 64 + c2 * 32, 0);
#pragma unroll
    for (int i = 0; i < 16; ++i) *(LAS u32x4*)(L + i * 8192 + tid * 16) = r[i];
    bar();
#pragma unroll
    for (int ks = 0; ks < 16; ++ks) {
      const u32 a = (u32)(w * 512 + (((ks >> 2) + ct) & 3) * 128 + (ks & 3) * 32 + 4 * g + ((lane >> 2) & 3)) * 32 + (lane & 3) * 8;
      const u32x2 lo = tr2(L + a), hi = tr2(L + a + 512);
      wf[c2 * 16 + ks] = (u32x4){lo[0], lo[1], hi[0], hi[1]};
    }
    bar();
  }
  const rsrc_t ro = mkrsrc(P.ws + OFF_O, 16u << 20), rmoe = mkrsrc(P.ws + OFF_MOE, 65536), rxn = mkrsrc(P.ws + OFF_XN, 2u << 20),
               rp = mkrsrc(P.ws + OFF_PM, 1u << 20), rw = mkrsrc(P.ws + OFF_VWM, 16u << 20);
  RT(5);
  LAS float* RED = (LAS float*)L;
  LAS u32* UP = (LAS u32*)(L + 32768);
  LAS int* IDX = (LAS int*)(L + 32768 + 1024);
  if (w == 0) {
    C.wait_ge(L_M3 + 2 * ct, 1); C.wait_ge(L_M3 + 2 * ct + 1, 1);
    UP[lane] = ld4s(rmoe, MOE_U + (lane >> 4) * 1024 + ct * 64 + tl * 4);
    UP[64 + lane] = ld4s(rmoe, MOE_U + (4 + (lane >> 4)) * 1024 + ct * 64 + tl * 4);
    if (lane < 8) IDX[lane] = (int)ld4s(rmoe, MOE_I + ct * 64 + lane * 4);
    if (lane < 16) UP[128 + lane] = 0;
  }
  bar();
  u64 segmask = 0;
#pragma unroll
  for (int i = 0; i < 8; ++i) segmask |= 1ull << ((uni(IDX[i]) >> 5) & 63);
  u32 seen = 0;
  bool ahead = false;
  const u32 h0 = ((0 + ct) & 3) * 8192, h1 = ((1 + ct) & 3) * 8192, h2 = ((2 + ct) & 3) * 8192, h3 = ((3 + ct) & 3) * 8192;
  u32x4 o0[8], o1[8];
  const int fl = (lane & 4) ? L_CH + (4 * w + (lane & 3)) * 4 + (ct >> 2) : L_O + 4 * w + (lane & 3);
  const int t = tid >> 4, cp = tid & 15;
  for (int s = par; s < NSEG; s += 2) {
    const int row = s * 32 + t;
    RTA(0);
    const u32 xx = *(const u32*)(P.x + (size_t)row * DM + ct * 32 + cp * 2);
    const u32 xn = ld4s(rxn, s * 32768 + xn_off(t, ct * 4 + (cp >> 2)) + (cp & 3) * 4);
    f32x4 a[4];
#pragma unroll
    for (int i = 0; i < 4; ++i) a[i] = (f32x4){0, 0, 0, 0};
    const u32 base = (u32)(s * 32 + 4 * w) * 8192;
    if (!ahead) {
      C.wait_lanes(fl, s + 1 + ((lane >> 2) & 1), seen);
#pragma unroll
      for (int j = 0; j < 8; ++j) { const u32 hb_ = base + (j < 4 ? h0 : h1) + (j & 3) * 1024; o0[j] = ld16o(ro, lane * 16, hb_); o1[j] = ld16o(ro, lane * 16, hb_ + 4096); }
    }
    RTA(1);
    const u32 hb = (u32)(s * 32 + 4 * w + g);
    u32x4 m0, m1, m2, m3;
    ahead = s + 2 < NSEG && __all(seen >= (u32)(s + 3 + ((lane >> 2) & 1)));
#pragma unroll
    for (int j = 0; j < 16; ++j) {
      __builtin_amdgcn_sched_barrier(0);
      a[0] = mfma32(wf[j], o0[j & 7], a[0]); a[1] = mfma32(wf[j], o1[j & 7], a[1]);
      a[2] = mfma32(wf[16 + j], o0[j & 7], a[2]); a[3] = mfma32(wf[16 + j], o1[j & 7], a[3]);
      __builtin_amdgcn_sched_barrier(0);
      if (j < 8) {
        const u32 hb_ = base + (j < 4 ? h2 : h3) + (j & 3) * 1024;
        o0[j] = ld16o(ro, lane * 16, hb_); o1[j] = ld16o(ro, lane * 16, hb_ + 4096);
        if (j == 3) {
          m0 = ld16s(rw, hb * 8192 + (ct * 32 + tl) * 16); m1 = ld16s(rw, hb * 8192 + (ct * 32 + 16 + tl) * 16);
          m2 = ld16s(rp, hb * 512 + tl * 16); m3 = ld16s(rp, hb * 512 + 256 + tl * 16);
        }
      } else if (ahead) {
        const int k = j - 8;
        const u32 hb_ = base + 524288 + (k < 4 ? h0 : h1) + (k & 3) * 1024;
        o0[k] = ld16o(ro, lane * 16, hb_); o1[k] = ld16o(ro, lane * 16, hb_ + 4096);
      }
      if (j == 0) RTA(2);
      if (j == 7) RTA(3);
      if (j == 15) RTA(4);
      if (j == 11) { a[0] = mfma32(m0, m2, a[0]); a[1] = mfma32(m0, m3, a[1]); a[2] = mfma32(m1, m2, a[2]); a[3] = mfma32(m1, m3, a[3]); }
    }
    if (!ahead && s + 2 < NSEG) seen = __hip_atomic_load(C.line(fl), RLX_AGENT);
#pragma unroll
    for (int i = 0; i < 4; ++i)
#pragma unroll
      for (int r = 0; r < 4; ++r) RED[((w * 4 + i) * 4 + r) * 64 + lane] = a[i][r];
    RTA(5);
    bar();
    RTA(6);
    {
      const int src = (((cp >> 3) * 2 + (t >> 4)) * 4 + (2 * cp & 3)) * 64 + ((cp >> 1) & 3) * 16 + (t & 15);
      float s0 = 0.f, s1 = 0.f;
#pragma unroll
      for (int i = 0; i < 8; ++i) { s0 += RED[i * 1024 + src]; s1 += RED[i * 1024 + src + 64]; }
      u32 up = 0;
      if ((segmask >> s) & 1) {
        int mi = 8;
#pragma unroll
        for (int i = 0; i < 8; ++i) mi = IDX[i] == row ? i : mi;
        up = UP[mi * 16 + cp];
      }
      const u32 zz = pk2(bflo(xn) * 0.0f + bflo(up), bfhi(xn) * 0.0f + bfhi(up));
      const u32 bb = pk2(bflo(xn) + bflo(zz), bfhi(xn) + bfhi(zz));
      const u32 aa = pk2(s0, s1);
      const u32 xa = pk2(bflo(xx) + bflo(aa), bfhi(xx) + bfhi(aa));
      *(u32*)(P.out + (size_t)row * DM + ct * 32 + cp * 2) = pk2(bflo(xa) + bflo(bb), bfhi(xa) + bfhi(bb));
    }
    RTA(7);
    bar();
    RTS(s);
  }
}

DEV u32 wave_max_u32(u32 v) {
#pragma unroll
  for (int o = 32; o >= 1; o >>= 1) { const u32 u = __shfl_xor(v, o); v = u > v ? u : v; }
  return v;
}
DEV void role_moe(const Params& P, Ctl& C, ldsp L, int e) {
  const int tid = threadIdx.x, lane = tid & 63, w = uni(tid >> 6), g = lane >> 4, n = lane & 15;
  const rsrc_t rmoe = mkrsrc(P.ws + OFF_MOE, 65536), rxn = mkrsrc(P.ws + OFF_XN, 2u << 20);
  {
    u32x4 wr[4];
    ln_load(P.wr, tid & 15, wr);
#pragma unroll
    for (int half = 0; half < 2; ++half) {
      u32x4 xv[4];
      const int row = e * 64 + half * 32 + (tid >> 4);
      ln_load(P.x + (size_t)row * DM, tid & 15, xv);
      const LnRow st = ln_stats(xv);
      float d = 0.f;
#pragma unroll
      for (int i = 0; i < 4; ++i) { const u32x4 y = ln_apply(xv[i], st);
        st16s(rxn, (e * 2 + half) * 32768 + xn_off(tid >> 4, (tid & 15) + 16 * i), y);
#pragma unroll
        for (int j = 0; j < 4; ++j) d += bflo(y[j]) * bflo(wr[i][j]) + bfhi(y[j]) * bfhi(wr[i][j]); }
      d = row16_sum(d);
      if ((tid & 15) == 0) ((LAS u16*)L)[half * 32 + (tid >> 4)] = (u16)(pk2(d, d) & 0xffffu);
    }
    drain();
    bar();
    if (tid < 32) { st4s(rmoe, MOE_R + e * 128 + tid * 4, ((LAS u32*)L)[tid]); drain(); if (tid == 0) C.post(L_M1 + e, 1); }
  }
  RT(1);
  u32x4 wg[16], wd[7];
  if (e < 28) {
    const u16* src = ((w & 1) ? P.wu : P.wg) + e * 64 + (w >> 1) * 16 + n;
#pragma unroll
    for (int ks = 0; ks < 16; ++ks)
#pragma unroll
      for (int j = 0; j < 4; ++j) { const size_t k = ks * 32 + 8 * g + 2 * j; wg[ks][j] = (u32)src[k * FF] | ((u32)src[(k + 1) * FF] << 16); }
  }
  {
    const u16* src = P.wd + e * 16 + n;
#pragma unroll
    for (int ks = 0; ks < 7; ++ks)
#pragma unroll
      for (int j = 0; j < 4; ++j) { const size_t k = w * 224 + ks * 32 + 8 * g + 2 * j; wd[ks][j] = (u32)src[k * DM] | ((u32)src[(k + 1) * DM] << 16); }
  }
  LAS u32* CAND = (LAS u32*)(L + 1024);
  LAS u32* TOP = (LAS u32*)(L + 2048);
  ldsp XS = L + 4096;
  for (int i = 0; i < 4; ++i) C.wait_ge(L_M1 + w * 4 + i, 1);
  {
    const u32x2 rv = ld8s(rmoe, MOE_R + tid * 8);
    u32 key[4];
#pragma unroll
    for (int i = 0; i < 4; ++i) {
      u32 b = (i & 1) ? (rv[i >> 1] >> 16) : (rv[i >> 1] & 0xffffu);
      if (b == 0x8000u) b = 0;
      const u32 ord = (b & 0x8000u) ? (~b & 0xffffu) : (b | 0x8000u);
      key[i] = (ord << 16) | (u32)(0xffff - (tid * 4 + i));
    }
    for (int it = 0; it < 8; ++it) {
      u32 m01 = key[0] > key[1] ? key[0] : key[1], m23 = key[2] > key[3] ? key[2] : key[3];
      const u32 mx = wave_max_u32(m01 > m23 ? m01 : m23);
#pragma unroll
      for (int i = 0; i < 4; ++i) key[i] = key[i] == mx ? 0u : key[i];
      if (lane == 0) CAND[w * 8 + it] = mx;
    }
    bar();
    u32 c = CAND[lane];
    for (int it = 0; it < 8; ++it) {
      const u32 mx = wave_max_u32(c);
      c = c == mx ? 0u : c;
      if (tid == 0) TOP[it] = mx;
    }
    bar();
  }
  RT(2);
  {
    const int r = tid >> 4;
    if (r < 16) {
      u32x4 xv[4];
      const int row = 0xffff - (int)(TOP[r & 7] & 0xffffu);
      ln_load(P.x + (size_t)row * DM, tid & 15, xv);
      const LnRow st = ln_stats(xv);
#pragma unroll
      for (int i = 0; i < 4; ++i) { u32x4 y = ln_apply(xv[i], st); if (r >= 8) y = (u32x4){0, 0, 0, 0}; *(LAS u32x4*)(XS + xn_off(r, (tid & 15) + 16 * i)) = y; }
    }
    bar();
  }
  LAS float* GU = (LAS float*)(L + 4096 + 16384);
  LAS u16* HT = (LAS u16*)(L + 4096 + 16384 + 4096);
  if (e < 28) {
    f32x4 a = {0, 0, 0, 0};
#pragma unroll
    for (int ks = 0; ks < 16; ++ks) a = mfma32(lds16(XS + xn_off(n, 4 * ks + g)), wg[ks], a);
    if (g < 2) {
#pragma unroll
      for (int r = 0; r < 4; ++r) GU[(w * 8 + 4 * g + r) * 16 + n] = a[r];
    }
    bar();
    {
      const int t = tid >> 6, f = tid & 63;
      const float gv = rbf(GU[((f >> 4) * 16 + t) * 16 + (f & 15)]), uv = rbf(GU[((f >> 4) * 16 + 8 + t) * 16 + (f & 15)]);
      const float sl = rbf(gv / (1.0f + __expf(-gv)));
      HT[t * 64 + f] = (u16)(pk2(sl * uv, 0.f) & 0xffffu);
    }
    bar();
    if (tid < 64) { st16s(rmoe, MOE_H + (tid >> 3) * (FF * 2) + e * 128 + (tid & 7) * 16, lds16((ldsp)HT + tid * 16)); drain(); if (tid == 0) C.post(L_M2 + e, 1); }
  }
  RT(3);
  {
    const int w0 = (w * 224) >> 6, w1 = (w * 224 + 223) >> 6;
    for (int i = w0; i <= w1; ++i) C.wait_ge(L_M2 + i, 1);
    f32x4 a = {0, 0, 0, 0};
    u32x4 hf[7];
#pragma unroll
    for (int ks = 0; ks < 7; ++ks) hf[ks] = ld16s(rmoe, MOE_H + (n & 7) * (FF * 2) + (w * 224 + ks * 32 + 8 * g) * 2);
#pragma unroll
    for (int ks = 0; ks < 7; ++ks) a = mfma32(hf[ks], wd[ks], a);
    bar();
    if (g < 2) {
#pragma unroll
      for (int r = 0; r < 4; ++r) GU[(w * 8 + 4 * g + r) * 16 + n] = a[r];
    }
    bar();
    if (tid < 64) {
      const int t = tid >> 3, cp = tid & 7;
      float s0 = 0.f, s1 = 0.f;
#pragma unroll
      for (int i = 0; i < 8; ++i) { s0 += GU[(i * 8 + t) * 16 + 2 * cp]; s1 += GU[(i * 8 + t) * 16 + 2 * cp + 1]; }
      const u32 key = TOP[t];
      const u32 ord = key >> 16, bits = (ord & 0x8000u) ? (ord & 0x7fffu) : (~ord & 0xffffu);
      const float rs = rbf(1.0f / (1.0f + __expf(-bflo(bits))));
      st4s(rmoe, MOE_U + t * 1024 + e * 32 + cp * 4, pk2(rbf(s0) * rs, rbf(s1) * rs));
      if (tid < 8) st4s(rmoe, MOE_I + e * 32 + tid * 4, 0xffffu - (TOP[tid] & 0xffffu));
      drain();
      if (tid == 0) C.post(L_M3 + e, 1);
      RT(4);
    }
  }
}

constexpr int C_MEM = 0, C_KF = 8192, C_KX = 12288, C_VX = 24576, C_QF = 32768, C_P = C_QF + 2048, C_ST = C_P + 1536,
              C_VP = C_ST + 1024, C_W = 40960, C_RAW = 32768;
static_assert(C_VP + 2048 <= C_W && C_W + 98304 <= LDS_BYTES && C_RAW + 131072 <= LDS_BYTES, "lds");

DEV int sp_row(int c4, int gb, int q) { return 16 * (2 * (c4 >> 1) + (gb >> 1)) + 8 * (q >> 1) + 2 * (2 * (gb & 1) + (c4 & 1)) + (q & 1); }
DEV int opq(int v) { asm volatile("" : "+v"(v)); return v; }
DEV void sched() { __builtin_amdgcn_sched_barrier(0); }
DEV f32x4 zero4() { f32x2 a, b; asm volatile("v_mov_b64 %0, 0\n\tv_mov_b64 %1, 0\n\ts_nop 1" : "=v"(a), "=v"(b)); return (f32x4){a[0], a[1], b[0], b[1]}; }
DEV u32 vconst(u32 c) { u32 r; asm volatile("v_mov_b32 %0, %1" : "=v"(r) : "s"(c)); return r; }
DEV void pin(f32x4& v) { asm volatile("" : "+v"(v)); }
DEV u32x8 wfrag_sparse(ldsp L, const u32 (&ad)[4]) {
  u32x8 f;
#pragma unroll
  for (int c4 = 0; c4 < 4; ++c4) { const u32x2 v = tr2(L + ad[c4]); f[2 * c4] = v[0]; f[2 * c4 + 1] = v[1]; }
  return f;
}
DEV u32x8 lds32(ldsp p) { const u32x4 a = lds16(p), b = lds16(p + 1024); return (u32x8){a[0], a[1], a[2], a[3], b[0], b[1], b[2], b[3]}; }
DEV void lds32w(ldsp p, u32x8 f) { *(LAS u32x4*)p = (u32x4){f[0], f[1], f[2], f[3]}; *(LAS u32x4*)(p + 1024) = (u32x4){f[4], f[5], f[6], f[7]}; }
DEV u32 half_sum_pk(f32x4 d) { float a = d[0], b = d[2], c = d[1], e = d[3]; swap32(a, b); swap32(c, e); return pk2(a + b, c + e); }
DEV u32x4 sent4() { u32 z = SENT; asm volatile("" : "+v"(z)); return (u32x4){z, z, z, z}; }
DEV bool all_set(u32x4 v) { const u32 a = v[0] < v[1] ? v[1] : v[0], b = v[2] < v[3] ? v[3] : v[2]; return __all((a < b ? b : a) != SENT); }
#define vmax(a, b) __builtin_amdgcn_fmed3f((a), (b), pinf)
#define STAMP(i) do { } while (0)

#define XT(i, extra) do { } while (0)
#define st16c st16o
DEV void role_chain(const Params& P, Ctl& C, unsigned char* xch, int id) {
  const ldsp L = reinterpret_cast<ldsp>(static_cast<uintptr_t>(C_MEM));
  float pinf = __builtin_inff(); asm volatile("" : "+s"(pinf));
  const int tid = threadIdx.x, lane = tid & 63, w = uni(tid >> 6), h = id >> 2, c4w = id & 3;
  u32x8 bq[8], bk[8], bw[8];
  u32x4 mv, mw;
  {
    u32x4 r0[16], r1[16];
    {
      u32x4 r2[16], r3[4];
      wraw_load(P.wv, h, tid, r2);
      const rsrc_t rs = mkrsrc(P.wo, 4u << 20);
#pragma unroll
      for (int i = 0; i < 4; ++i) r3[i] = __builtin_amdgcn_raw_buffer_load_b128(rs, (u32)(tid >> 4) * 1024 + (tid & 15) * 16, h * 131072 + i * 32768 + c4w * 256, 0);
      wraw_load(P.wq, h, tid, r0);
      wraw_load(P.wk, h, tid, r1);
#pragma unroll
      for (int i = 0; i < 16; ++i) { const int row = i * 32 + (tid >> 4); *(LAS u32x4*)(L + row * 256 + (((tid & 15) ^ ((row & 1) | ((row >> 1) & 14))) << 4)) = r2[i]; }
#pragma unroll
      for (int i = 0; i < 4; ++i) { const int row = i * 32 + (tid >> 4), c = tid & 15; *(LAS u32x4*)(L + 131072 + row * 256 + (((c >> 1) ^ wswz<0>(row)) << 5) + (c & 1) * 16) = r3[i]; }
    }
    bar();
    {
      u32x4 wof[4];
#pragma unroll
      for (int ks = 0; ks < 4; ++ks) wof[ks] = wfrag_dense(L + 131072, lane, w, ks);
      const int tl = lane & 15, g = lane >> 4;
#pragma unroll
      for (int c4 = 0; c4 < 4; ++c4) {
        const int row = sp_row(c4, tl >> 2, tl & 3), f = (row & 1) | ((row >> 1) & 14);
        u32 a4[4];
#pragma unroll
        for (int ks = 0; ks < 4; ++ks) a4[ks] = opq(row * 256 + (((4 * ks + g) ^ f) << 4));
#pragma unroll
        for (int i = 0; i < 8; ++i) {
          f32x4 d = {0, 0, 0, 0};
#pragma unroll
          for (int ks = 0; ks < 4; ++ks) d = mfma32(lds16(L + a4[ks] + (i & 3) * 16384 + (i >> 2) * 65536), wof[ks], d);
          const u32 lo = opq(pk2(d[0], d[1])), hi = opq(pk2(d[2], d[3]));
          bw[i][2 * c4] = lo; bw[i][2 * c4 + 1] = hi;
          sched();
        }
      }
    }
    bar();
    const ldsp R = L + C_RAW;
    u32 ad[4], ah[4];
    {
      const int ln = opq(lane);
#pragma unroll
      for (int c = 0; c < 4; ++c) {
        const int row = sp_row(c, ln >> 4, (ln >> 2) & 3);
        ad[c] = opq(C_RAW + row * 256 + ((w ^ wswz<1>(row)) << 5) + (ln & 3) * 8); ah[c] = opq(ad[c] + 65536);
      }
    }
    wraw_store<1>(R, opq(tid), r0);
    bar();
#pragma unroll
    for (int i = 0; i < 4; ++i) { bq[i] = wfrag_sparse(L + i * 16384, ad); bq[4 + i] = wfrag_sparse(L + i * 16384, ah); sched(); }
    bar();
    wraw_store<1>(R, opq(tid), r1);
    bar();
#pragma unroll
    for (int i = 0; i < 4; ++i) { bk[i] = wfrag_sparse(L + i * 16384, ad); bk[4 + i] = wfrag_sparse(L + i * 16384, ah); sched(); }
    bar();
#pragma unroll
    for (int i = 0; i < 4; ++i) lds32w(L + C_W + w * 8192 + lane * 16 + i * 2048, bw[4 + i]);
    const int t8 = tid & 255;
    const u16* mp = P.mem0 + (2 * (t8 & 3)) * DM + (t8 >> 2) * 4;
    const u32x2 m0 = *(const u32x2*)mp, m1 = *(const u32x2*)(mp + DM), m2 = *(const u32x2*)(mp + 256), m3 = *(const u32x2*)(mp + DM + 256);
    mv = (u32x4){m0[0], m0[1], m1[0], m1[1]}; mw = (u32x4){m2[0], m2[1], m3[0], m3[1]};
  }
  RT(1);
  const rsrc_t rx = mkrsrc(P.ws, 160u << 20);
  const u32 xo = (u32)(xch - P.ws);
  const u32 kb0 = (u32)OFF_KX + h * 8192, vb0 = (u32)OFF_VW + h * 32768 + c4w * 8192, mb0 = (u32)OFF_KM + h * 2048, wb0 = (u32)OFF_VWM + h * 8192 + c4w * 2048;
  u64 msel; u32 pub, own0, own1; u64 flipm;
  {
    u32 t0 = 0, t1 = 0, t2 = 0, n = 0;
    GAS u32* T = C.line(L_TBL);
    for (;;) {
      t0 = __hip_atomic_load(T + lane, RLX_AGENT); t1 = __hip_atomic_load(T + 64 + lane, RLX_AGENT); t2 = __hip_atomic_load(T + 128 + lane, RLX_AGENT);
      if (__all(t0 != 0 && t1 != 0 && t2 != 0) || C.dead) break;
      __builtin_amdgcn_s_sleep(8);
      if (C.spin(n)) break;
    }
    msel = __ballot(t0 == 2);
    const u64 s0 = __ballot(t1 == 2), s1 = __ballot(t2 == 2);
    const int r = 32 * c4w + 4 * w + (lane >> 4), b = r >> 1;
    pub = opq(b * 16384 + (u32)((s0 >> b) & 1) * 8192 + (r & 1) * 4096 + h * 128 + (lane & 7) * 16);
    flipm = __ballot((((s0 ^ s1) >> b) & 1) != 0);
    own0 = xo + (64 + (id >> 1)) * 16384 + (u32)((s0 >> (id >> 1)) & 1) * 8192 + (id & 1) * 4096;
    own1 = xo + (128 + (id >> 1)) * 16384 + (u32)((s1 >> (id >> 1)) & 1) * 8192 + (id & 1) * 4096;
  }
  RT(2);
  const int sidx = (lane & 8) ? 0xEEEE : 0x4444;
  const u32 t16 = tid * 16;
  u32 seenK = 0, seenV = 0;
  if (w >= 4) {
    seenK = C.wait_ge(L_KV + h, 1); seenV = C.wait_ge(L_KV + 32 + h, 1);
    const u32 o = t16 - 4096;
    const u32x4 k0 = ld16o(rx, o, kb0), k1 = ld16o(rx, o, kb0 + 4096);
    const u32x4 v0 = ld16o(rx, o, vb0), v1 = ld16o(rx, o, vb0 + 4096);
    *(LAS u32x4*)(L + C_KX + o) = k0; *(LAS u32x4*)(L + C_KX + 4096 + o) = k1;
    *(LAS u32x4*)(L + C_VX + o) = v0; *(LAS u32x4*)(L + C_VX + 4096 + o) = v1;
  }
  const u32 a_mw = opq((tid >> 3) * 128 + (tid & 3) * 32 + ((tid >> 2) & 1) * 8);
  const u32 l16p = opq(lane * 16);
  const u32 tso = xo + (u32)(msel & 1) * 8192;
  u32 tg2 = 0, nest = 0, nest2 = 0, tprev2 = 0;

  auto put_mem = [&](const u32x4& a, const u32x4& b) __attribute__((always_inline)) {
    *(LAS u32x2*)(L + C_MEM + a_mw) = (u32x2){a[0], a[1]}; *(LAS u32x2*)(L + C_MEM + a_mw + 16) = (u32x2){a[2], a[3]};
    *(LAS u32x2*)(L + C_MEM + 4096 + a_mw) = (u32x2){b[0], b[1]}; *(LAS u32x2*)(L + C_MEM + 4096 + a_mw + 16) = (u32x2){b[2], b[3]};
  };
  if (w < 4) put_mem(mv, mw);
  auto step_loop = [&](auto role_c) __attribute__((always_inline)) {
  constexpr int ROLE = decltype(role_c)::value;
  f32x4 aq = zero4(), ak = zero4();
  for (int s = 0; s < NSEG - 1; ++s) {
    STAMP(1);
    RTS(s);
    bar();
    STAMP(8);
    u32x4 af[8];
#pragma unroll
    for (int i = 0; i < 3; ++i) af[i] = lds16(L + C_MEM + i * 1024 + l16p);
    sched();
    const u32 kfo = (s & 1) * 12288;
    u32 a_fr = 0, a_kr = 0, aq_ = 0, ak_ = 0;
#pragma unroll
    for (int i = 0; i < 8; ++i) {
      sched();
      if (i + 3 < 8) af[i + 3] = lds16(L + C_MEM + (i + 3) * 1024 + l16p);
      sched();
      aq = smf(af[i], bq[i], aq, sidx); ak = smf(af[i], bk[i], ak, sidx);
      pin(aq); pin(ak);
      if (ROLE == 0 && i == 1) { const u32 l16 = opq(l16p); a_fr = opq((l16 >> 8) * 128 + (l16 & 112)); a_kr = opq(w == 0 ? a_fr + kfo : w * 4096 + l16); }
      if (i == (ROLE == 2 ? 5 : 3)) {
        const u32 l16 = opq(l16p);
        const u32 a_fw = (w & 1) * 8 + ((l16 >> 6) & 3) * 128 + ((l16 >> 8) & 1) * 64 + (l16 >> 9) * 32 + ((l16 >> 4) & 3) * 2;
        aq_ = opq(a_fw + (w >> 1) * 512); ak_ = opq(a_fw + (w >> 1) * 1024 + kfo);
      }
    }
    sched();
    {
      const u32 qp = half_sum_pk(aq);
      *(LAS u16*)(L + C_QF + aq_) = (u16)qp; *(LAS u16*)(L + C_QF + aq_ + 16) = (u16)(qp >> 16);
      const u32 kp = half_sum_pk(ak);
      *(LAS u16*)(L + C_KF + ak_) = (u16)kp; *(LAS u16*)(L + C_KF + ak_ + 16) = (u16)(kp >> 16);
    }
    f32x4 av = zero4();
    sched();
    if (ROLE == 0) __builtin_amdgcn_s_setprio(3);
    STAMP(2);
    bar();
    STAMP(9);
    u32x4 vxf, k0, k1, v0, v1;
    u32 a_p, a_p0, a_st, vpk = 0, o = 0;
    if (ROLE == 0) {
      u32x4 qf[4], kf[4];
#pragma unroll
      for (int p = 0; p < 4; ++p) { qf[p] = lds16(L + C_QF + p * 512 + a_fr); kf[p] = lds16(L + C_KF + p * 1024 + a_kr); }
      sched();
      const u32 l16 = opq(l16p);
      const u32 a_pw = opq((l16 >> 1) + w * 512);
      const u32 a_sw = opq((l16 & 240) + w * 4);
      const ldsp LW = L + opq(C_W + w * 8192 + l16);
      av = smf(af[0], bw[0], av, sidx); av = smf(af[1], bw[1], av, sidx); av = smf(af[2], bw[2], av, sidx);
      pin(av);
      sched();
      u32x8 su[4];
      su[0] = lds32(LW);
      vxf = lds16(L + C_VX + t16);
      a_p = opq(l16 >> 1); a_p0 = opq(((l16 >> 1) & 255) + (l16 >> 9) * 4); a_st = opq(l16 & 112);
      sched();
      f32x4 sc = {0, 0, 0, 0};
#pragma unroll
      for (int p = 0; p < 4; ++p) sc = mfma32(kf[p], qf[p], sc);
      pin(sc);
      sched();
      su[1] = lds32(LW + 2048); su[2] = lds32(LW + 4096);
      av = smf(af[3], bw[3], av, sidx); pin(av);
      sched();
      const float cs = 0.08838834764831845f * 1.4426950408889634f;
      float mx = vmax(vmax(sc[0], sc[1]), vmax(sc[2], sc[3]));
      { float a = mx, b = mx; swap16(a, b); mx = vmax(a, b); }
      sched();
      av = smf(af[4], su[0], av, sidx); pin(av);
      su[3] = lds32(LW + 6144);
      sched();
      { float a = mx, b = mx; swap32(a, b); mx = vmax(a, b); }
      mx *= cs;
      float e[4];
#pragma unroll
      for (int r = 0; r < 4; ++r) e[r] = __builtin_amdgcn_exp2f(__builtin_fmaf(sc[r], cs, -mx));
      *(LAS float*)(L + C_ST + a_sw) = mx;
      *(LAS u32x2*)(L + C_P + a_pw) = (u32x2){pk2(e[0], e[1]), pk2(e[2], e[3])};
      sched();
      av = smf(af[5], su[1], av, sidx); av = smf(af[6], su[2], av, sidx); av = smf(af[7], su[3], av, sidx); pin(av);
    } else {
#pragma unroll
      for (int i = 0; i < 4; ++i) av = smf(af[i], bw[i], av, sidx);
      pin(av);
      sched();
      if (ROLE == 2) {
        if (seenK < (u32)s + 2) seenK = C.wait_ge(L_KV + h, s + 2);
        if (seenV < (u32)s + 2) seenV = C.wait_ge(L_KV + 32 + h, s + 2);
        o = opq(t16) - 4096;
        const u32 kb = kb0 + (s + 1) * 262144, vb = vb0 + (s + 1) * 1048576;
        k0 = ld16o(rx, o, kb); k1 = ld16o(rx, o, kb + 4096); v0 = ld16o(rx, o, vb); v1 = ld16o(rx, o, vb + 4096);
      }
      vxf = lds16(L + C_VX + t16);
      sched();
#pragma unroll
      for (int i = 4; i < 8; ++i) av = smf(af[i], bw[i], av, sidx);
      const u32 l16 = opq(l16p);
      a_p = opq(l16 >> 1); a_p0 = opq(((l16 >> 1) & 255) + (l16 >> 9) * 4); a_st = opq(l16 & 112);
      vpk = opq(half_sum_pk(av));
      sched();
      if (ROLE == 1) {
        if (c4w == 0) {
          const u32 a = C_KF + kfo + (l16 >> 9) * 1024 + (l16 & 511);
          st16c(lds16(L + a), rx, l16, mb0 + s * 65536); st16c(lds16(L + a + 2048), rx, l16, mb0 + s * 65536 + 1024);
        }
        if (s > 0) {
          const u32 so = (s & 1) ? own0 : own1;
          const u32x4 z = sent4();
          st16c(z, rx, l16, so); st16c(z, rx, l16, so + 1024); st16c(z, rx, l16, so + 2048); st16c(z, rx, l16, so + 3072);
          const u32 a = C_VP + (l16 >> 8) * 256 + ((l16 >> 4) & 15) * 4;
          const u32x4 d0 = {*(LAS u32*)(L + a), *(LAS u32*)(L + a + 128), *(LAS u32*)(L + a + 64), *(LAS u32*)(L + a + 192)};
          const u32x4 d1 = {*(LAS u32*)(L + a + 1024), *(LAS u32*)(L + a + 1152), *(LAS u32*)(L + a + 1088), *(LAS u32*)(L + a + 1216)};
          st16c(d0, rx, l16, wb0 + (s - 1) * 262144); st16c(d1, rx, l16, wb0 + (s - 1) * 262144 + 1024);
        }
      }
    }
    const u32 o1c = vconst(0x3f803f80u), zc = vconst(0u);
    STAMP(3);
    bar();
    STAMP(10);
    {
      const u32x2 p1 = lds8(L + C_P + 512 + a_p), p2 = lds8(L + C_P + 1024 + a_p);
      const u32 p0 = *(LAS u32*)(L + C_P + a_p0);
      const f32x4 sm = *(LAS f32x4*)(L + C_ST + a_st);
      sched();
      if (ROLE == 0) vpk = half_sum_pk(av);
      const f32x4 z = {0, 0, 0, 0};
      const u32x2 one = {o1c, o1c}, vp2 = {vpk, zc}, pz = {p0, zc};
      const u32 pso = xo + ((s & 1) ? 128 * 16384 : 64 * 16384);
      sched();
      f32x4 l1 = mfma16(one, p1, z), l2 = mfma16(one, p2, z), l0 = mfma16(one, pz, z);
      pin(l1); pin(l2); pin(l0);
      sched();
      const float m = vmax(vmax(sm[0], sm[1]), sm[2]);
      f32x4 o1 = mfma16((u32x2){vxf[0], vxf[1]}, p1, z); pin(o1);
      const float c0 = __builtin_amdgcn_exp2f(sm[0] - m), c1 = __builtin_amdgcn_exp2f(sm[1] - m);
      f32x4 o2 = mfma16((u32x2){vxf[2], vxf[3]}, p2, z); pin(o2);
      const float c2 = __builtin_amdgcn_exp2f(sm[2] - m);
      f32x4 o0 = mfma16(vp2, pz, z); pin(o0);
      sched();
      const float inv = __builtin_amdgcn_rcpf(c0 * l0[0] + c1 * l1[0] + c2 * l2[0]);
      const f32x4 on = o1 * (c1 * inv) + o2 * (c2 * inv) + o0 * (c0 * inv);
      st16c(__builtin_bit_cast(u32x4, on), rx, pub, pso);
      asm volatile("s_mov_b64 exec, %1\n\tv_xor_b32 %0, 0x2000, %0\n\ts_mov_b64 exec, -1" : "+v"(pub) : "s"(flipm));
      XT(0, 0);
      *(LAS u32*)(L + C_VP + (t16 >> 2)) = vpk;
      STAMP(5);
      sched();
    }
    if (ROLE == 0) __builtin_amdgcn_s_setprio(0);
    aq = zero4(); ak = zero4();
    if (ROLE == 2) {
      if (seenK < (u32)s + 6 && seenK < (u32)NSEG) seenK = uni(__hip_atomic_load(C.line(L_KV + h), RLX_AGENT));
      if (seenV < (u32)s + 6 && seenV < (u32)NSEG) seenV = uni(__hip_atomic_load(C.line(L_KV + 32 + h), RLX_AGENT));
      bar();
      *(LAS u32x4*)(L + C_KX + o) = k0; *(LAS u32x4*)(L + C_KX + 4096 + o) = k1;
      *(LAS u32x4*)(L + C_VX + o) = v0; *(LAS u32x4*)(L + C_VX + 4096 + o) = v1;
    } else {
      const u32 mo = xo + (u32)(s + 1) * 16384 + (u32)((msel >> (s + 1)) & 1) * 8192;
      u32 n = 0;
      if (ROLE == 1) {
        const u32 so = (s & 1) ? own1 : own0;
        const u32 l16 = opq(l16p);
        const u32 ao = opq(((l16 >> 4) & 7) * 8 + (l16 >> 9) * 4);
        u32x4 p0, p1, p2, p3;
        for (int d = 0; d < PD2; ++d) __builtin_amdgcn_s_sleep(1);
        cbar();
        XT(1, 0);
        p0 = ld16o(rx, l16, so); p1 = ld16o(rx, l16, so + 1024); p2 = ld16o(rx, l16, so + 2048); p3 = ld16o(rx, l16, so + 3072);
        if (__builtin_expect(!all_set(__builtin_elementwise_max(__builtin_elementwise_max(p0, p1), __builtin_elementwise_max(p2, p3))), 0)) {
          while (!C.dead) {
            cbar();
            p0 = ld16o(rx, l16, so); p1 = ld16o(rx, l16, so + 1024); p2 = ld16o(rx, l16, so + 2048); p3 = ld16o(rx, l16, so + 3072);
            if (all_set(__builtin_elementwise_max(__builtin_elementwise_max(p0, p1), __builtin_elementwise_max(p2, p3))) || C.spin(n)) break;
          }
        }
        STAMP(6);
        XT(2, n);
        const f32x4 t = (__builtin_bit_cast(f32x4, p0) + __builtin_bit_cast(f32x4, p1)) + (__builtin_bit_cast(f32x4, p2) + __builtin_bit_cast(f32x4, p3));
        float a0 = t[0], a1 = t[1], a2 = t[2], a3 = t[3];
        swap32(a0, a2); swap32(a1, a3);
        float u0 = a0 + a2, u1 = a1 + a3;
        swap16(u0, u1);
        u0 += u1;
        u0 += ror16<8>(u0);
        float b0 = u0, b1 = u0;
        swap16(b0, b1);
        __builtin_amdgcn_raw_buffer_store_b32(nosent(pk2(b0, b1)), rx, ao, mo + id * 64, SC1);
        if (id == 0) __builtin_amdgcn_raw_buffer_store_b32(nosent((u32)__builtin_amdgcn_s_memrealtime()), rx, 0, tso + s * 128, SC1);
        if (lane == 0) C.post(L_CH + id, s + 1);
      }
      bar();
      STAMP(7);
      XT(3, 0);
      n = 0;
      {
        const u32 now = (u32)__builtin_amdgcn_s_memrealtime();
        if (s >= 2) {
          tg2 += nest + DELTA2;
          int k = ((int)(tg2 - now) * 3) >> 3;
          k = k < PD1MIN ? PD1MIN : (k > 64 ? 64 : k);
          for (int d = 0; d < k; ++d) __builtin_amdgcn_s_sleep(1);
        } else {
          for (int d = 0; d < PD1; ++d) __builtin_amdgcn_s_sleep(1);
        }
      }
      cbar();
      XT(4, 0);
      u32 ts = __builtin_amdgcn_raw_buffer_load_b32(rx, 0, tso + s * 128, SC1);
      u32x4 ma = ld16o(rx, t16, mo), mb = ld16o(rx, t16, mo + 4096);
      {
        u32x4 mm = __builtin_elementwise_max(ma, mb); mm[0] = mm[0] > ts ? mm[0] : ts;
        if (__builtin_expect(!all_set(mm), 0)) {
          while (!C.dead) {
            cbar();
            ts = __builtin_amdgcn_raw_buffer_load_b32(rx, 0, tso + s * 128, SC1);
            ma = ld16o(rx, t16, mo); mb = ld16o(rx, t16, mo + 4096);
            mm = __builtin_elementwise_max(ma, mb); mm[0] = mm[0] > ts ? mm[0] : ts;
            if (all_set(mm) || C.spin(n)) break;
          }
        }
      }
      put_mem(ma, mb);
      sched();
      XT(5, n);
      ts = uni(ts);
      if (s >= 2) { const u32 late = ts + DELTA2 - tg2; if ((int)late > 0) tg2 += late; }
      if (s >= 1) { const u32 nn = ts - tprev2; nest = (s >= 3 && nest2 < nn) ? nest2 : nn; nest2 = nn; }
      if (s < 2) tg2 = ts + DELTA2;
      tprev2 = tg2;
    }
  }
  };
  if (w < 3) step_loop(std::integral_constant<int, 0>());
  else if (w == 3) step_loop(std::integral_constant<int, 1>());
  else step_loop(std::integral_constant<int, 2>());
  {
    const int s = NSEG - 1;
    RTS(s);
    bar();
    if (w < 2) {
      const u32 a = C_VP + (t16 >> 8) * 256 + ((t16 >> 4) & 15) * 4;
      const u32x4 d = {*(LAS u32*)(L + a), *(LAS u32*)(L + a + 128), *(LAS u32*)(L + a + 64), *(LAS u32*)(L + a + 192)};
      st16o(d, rx, t16, wb0 + (s - 1) * 262144);
    }
    const u32 l16 = l16p;
    const ldsp LW = L + C_W + w * 8192 + l16;
    f32x4 ak = {0, 0, 0, 0}, av = {0, 0, 0, 0};
#pragma unroll
    for (int i = 0; i < 8; ++i) {
      const u32x4 af = lds16(L + C_MEM + i * 1024 + l16);
      ak = smf(af, bk[i], ak, sidx);
      av = smf(af, i < 4 ? bw[i & 3] : lds32(LW + (i & 3) * 2048), av, sidx);
    }
    const u32 a_fw = (u32)(w >> 1) * 1024 + (w & 1) * 8 + ((l16 >> 6) & 3) * 128 + ((l16 >> 8) & 1) * 64 + (l16 >> 9) * 32 + ((l16 >> 4) & 3) * 2;
    const u32 kp = half_sum_pk(ak), vpk = half_sum_pk(av);
    *(LAS u16*)(L + C_KF + 12288 + a_fw) = (u16)kp; *(LAS u16*)(L + C_KF + 12288 + a_fw + 16) = (u16)(kp >> 16);
    __builtin_amdgcn_raw_buffer_store_b32(vpk, rx, (l16 & 240) + ((l16 >> 8) & 1) * 8 + (l16 >> 9) * 4, wb0 + s * 262144 + w * 256, SC1);
    bar();
    if (c4w == 0 && w >= 6) st16o(lds16(L + C_KF + 12288 + (w - 6) * 2048 + (l16 >> 9) * 1024 + (l16 & 511)), rx, t16 - 6144, mb0 + s * 65536);
    drain();
    bar();
    if (tid == 0) C.post(L_CH + id, NSEG + 1);
  }
}

__global__ void __launch_bounds__(512, 2) rmt_kernel(Params P) {
  extern __shared__ __attribute__((aligned(16))) unsigned char smem[];
  ldsp L = (ldsp)smem;
  Ctl C; C.c = (GAS u32*)P.ws; C.dead = false;
  unsigned char* xch = (unsigned char*)(((uintptr_t)(P.ws + OFF_XCH) + 16383) & ~(uintptr_t)16383);
  if (threadIdx.x == 0) {
    const u32 x = (u32)__builtin_amdgcn_s_getreg(20 | (3 << 11)) & 7u;
    u32 t = __hip_atomic_fetch_add(C.line(L_TICKET + x), 1u, RLX_AGENT);
    u32 slot = x * 32 + t;
    if (t >= 32) {
      const u32 o = __hip_atomic_fetch_add(C.line(L_OVF), 1u, RLX_AGENT);
      u32 cnt[8], n = 0;
      for (;;) {
        u32 tot = 0;
        for (int i = 0; i < 8; ++i) { cnt[i] = __hip_atomic_load(C.line(L_TICKET + i), RLX_AGENT); tot += cnt[i]; }
        if (tot >= 256) break;
        __builtin_amdgcn_s_sleep(8);
        if (++n > (1u << 20)) break;
      }
      u32 k = o; slot = 255;
      for (int i = 0; i < 8; ++i) { const u32 fr = cnt[i] < 32 ? 32 - cnt[i] : 0; if (k < fr) { slot = i * 32 + cnt[i] + k; break; } k -= fr; }
    }
    *(LAS u32*)L = slot;
  }
  bar();
  const int slot = uni(*(LAS u32*)L);
  bar();
  const int x = slot >> 5, t = slot & 31;
  RT(0);
  if (x < 4) role_chain(P, C, xch, slot);
  else {
    const int i = (x - 4) * 8 + (t >> 2);
    if ((t & 3) == 0) role_k(P, C, L, i);
    else if ((t & 3) == 1) role_v(P, C, L, i);
    else if ((t & 3) == 2) role_attn(P, C, L, xch, i, (((u32)__builtin_amdgcn_s_getreg(20 | (3 << 11)) >> 2) & 1u) != 0u);
    else { role_moe(P, C, L, i); role_out(P, C, L, i >> 1, i & 1); }
  }
}

extern "C" void kernel_launch(void* const* d_in, const int* in_sizes, int n_in,
                              void* d_out, int out_size, void* d_ws, size_t ws_size,
                              hipStream_t stream) {
  Params p;
  p.x = (const u16*)d_in[0]; p.mem0 = (const u16*)d_in[1]; p.wq = (const u16*)d_in[2]; p.wk = (const u16*)d_in[3];
  p.wv = (const u16*)d_in[4]; p.wo = (const u16*)d_in[5]; p.wr = (const u16*)d_in[8]; p.wg = (const u16*)d_in[9];
  p.wu = (const u16*)d_in[10]; p.wd = (const u16*)d_in[11];
  p.out = (u16*)d_out; p.ws = (unsigned char*)d_ws;
  hipFuncSetAttribute((const void*)rmt_kernel, hipFuncAttributeMaxDynamicSharedMemorySize, LDS_BYTES);
  hipMemsetAsync(d_ws, 0, CTRL_BYTES, stream);
  hipLaunchKernelGGL(rmt_kernel, dim3(256), dim3(512), LDS_BYTES, stream, p);
}
```

```cpp
#include <hip/hip_runtime.h>
#include <hip/hip_bf16.h>
#include <stdint.h>
#include <type_traits>

typedef unsigned short u16;
typedef unsigned int u32;
typedef unsigned long long u64;
typedef __bf16 bf16x2 __attribute__((ext_vector_type(2)));
typedef __bf16 bf16x8 __attribute__((ext_vector_type(8)));
typedef __bf16 bf16x16 __attribute__((ext_vector_type(16)));
typedef short s16x4 __attribute__((ext_vector_type(4)));
typedef float f32x2 __attribute__((ext_vector_type(2)));
typedef float f32x4 __attribute__((ext_vector_type(4)));
typedef u32 u32x2 __attribute__((ext_vector_type(2)));
typedef u32 u32x4 __attribute__((ext_vector_type(4)));
typedef u32 u32x8 __attribute__((ext_vector_type(8)));
#define LAS __attribute__((address_space(3)))
#define GAS __attribute__((address_space(1)))
typedef LAS unsigned char* ldsp;
typedef __amdgpu_buffer_rsrc_t rsrc_t;
#define DEV __device__ __forceinline__
#define RLX_AGENT __ATOMIC_RELAXED, __HIP_MEMORY_SCOPE_AGENT
#define SC1 16

constexpr int NSEG = 64, DM = 512, NH = 32, FF = 1792;
constexpr u32 SENT = 0xFFFFFFFFu;
constexpr size_t CTRL_BYTES = 49152;
constexpr int L_TICKET = 0, L_OVF = 8, L_ABORT = 9, L_KV = 16, L_O = 80, L_M1 = 112, L_M2 = 144, L_M3 = 176, L_TBL = 208, L_CH = 216, L_O2 = 344;
constexpr size_t OFF_XCH = 1u << 20;
constexpr size_t OFF_KX = 8u << 20;
constexpr size_t OFF_VX = 24u << 20;
constexpr size_t OFF_O = 40u << 20;
constexpr size_t OFF_KM = 56u << 20;
constexpr size_t OFF_PM = 60u << 20;
constexpr size_t OFF_MOE = 64u << 20;
constexpr size_t OFF_XN = 68u << 20;
constexpr size_t OFF_VWM = 80u << 20;
constexpr size_t OFF_VW = 96u << 20;
constexpr u32 MOE_R = 0, MOE_H = 4096, MOE_U = 4096 + 32768, MOE_I = 4096 + 32768 + 8192;
constexpr int LDS_BYTES = 160 * 1024;
constexpr int S_HELPA = 50;

constexpr int DELTA2 = 12;
constexpr int EXT0 = 4;
constexpr int EMA = 3, LATE = 4, SLIP = 16, NMAX0 = 235, ASTART = 4, ADAPT = 1, TSWG = 0;
constexpr int SLNUM = 11;
constexpr int PD1MIN = 2, PD1 = 14, PD2 = 8;
constexpr int HOLD = 800;

struct Params {
  const u16* x; const u16* mem0; const u16* wq; const u16* wk; const u16* wv; const u16* wo;
  const u16* wr; const u16* wg; const u16* wu; const u16* wd;
  u16* out; unsigned char* ws;
};

DEV float bflo(u32 u) { return __uint_as_float(u << 16); }
DEV float bfhi(u32 u) { return __uint_as_float(u & 0xffff0000u); }
DEV u32 pk2(float a, float b) { f32x2 f = {a, b}; return __builtin_bit_cast(u32, __builtin_convertvector(f, bf16x2)); }
DEV float rbf(float a) { return bflo(pk2(a, a)); }
DEV u32 nosent(u32 v) { return v < 0xFFFFFFFEu ? v : 0xFFFFFFFEu; }
template <int N> DEV float ror16(float v) { return __int_as_float(__builtin_amdgcn_update_dpp(0, __float_as_int(v), 0x120 + N, 0xf, 0xf, false)); }
DEV float row16_sum(float v) { v += ror16<8>(v); v += ror16<4>(v); v += ror16<2>(v); v += ror16<1>(v); return v; }
DEV void swap32(float& a, float& b) { auto r = __builtin_amdgcn_permlane32_swap(__float_as_uint(a), __float_as_uint(b), false, false); a = __uint_as_float(r[0]); b = __uint_as_float(r[1]); }
DEV void swap16(float& a, float& b) { auto r = __builtin_amdgcn_permlane16_swap(__float_as_uint(a), __float_as_uint(b), false, false); a = __uint_as_float(r[0]); b = __uint_as_float(r[1]); }
DEV float xmax_g(float v) { float a = v, b = v; swap16(a, b); v = fmaxf(a, b); a = v; b = v; swap32(a, b); return fmaxf(a, b); }
DEV float xsum_g(float v) { float a = v, b = v; swap16(a, b); v = a + b; a = v; b = v; swap32(a, b); return a + b; }
DEV s16x4 trread(ldsp p) { return __builtin_amdgcn_ds_read_tr16_b64_v4i16((LAS s16x4*)p); }
DEV u32x2 tr2(ldsp p) { return __builtin_bit_cast(u32x2, trread(p)); }
DEV u32x4 lds16(ldsp p) { return *(LAS u32x4*)p; }
DEV u32x2 lds8(ldsp p) { return *(LAS u32x2*)p; }
DEV f32x4 mfma32(u32x4 a, u32x4 b, f32x4 c) { return __builtin_amdgcn_mfma_f32_16x16x32_bf16(__builtin_bit_cast(bf16x8, a), __builtin_bit_cast(bf16x8, b), c, 0, 0, 0); }
DEV f32x4 mfma16(u32x2 a, u32x2 b, f32x4 c) { return __builtin_amdgcn_mfma_f32_16x16x16bf16_1k(__builtin_bit_cast(s16x4, a), __builtin_bit_cast(s16x4, b), c, 0, 0, 0); }
DEV f32x4 smf(u32x4 a, u32x8 b, f32x4 c, int idx) { return __builtin_amdgcn_smfmac_f32_16x16x64_bf16(__builtin_bit_cast(bf16x8, a), __builtin_bit_cast(bf16x16, b), c, idx, 0, 0); }
DEV rsrc_t mkrsrc(const void* p, u32 bytes) { return __builtin_amdgcn_make_buffer_rsrc((void*)p, 0, bytes, 0x00020000); }
DEV u32x4 ld16s(rsrc_t r, u32 off) { return __builtin_amdgcn_raw_buffer_load_b128(r, off, 0, SC1); }
DEV u32x2 ld8s(rsrc_t r, u32 off) { return __builtin_amdgcn_raw_buffer_load_b64(r, off, 0, SC1); }
DEV u32 ld4s(rsrc_t r, u32 off) { return __builtin_amdgcn_raw_buffer_load_b32(r, off, 0, SC1); }
DEV void st16o(u32x4 v, rsrc_t r, u32 off, u32 so) { __builtin_amdgcn_raw_buffer_store_b128(v, r, off, so, SC1); asm volatile("s_nop 1" :: "v"(v)); }
DEV void st16s(rsrc_t r, u32 off, u32x4 v) { st16o(v, r, off, 0); }
DEV u32x4 ld16o(rsrc_t r, u32 off, u32 so) { return __builtin_amdgcn_raw_buffer_load_b128(r, off, so, SC1); }
DEV void st8s(rsrc_t r, u32 off, u32x2 v) { __builtin_amdgcn_raw_buffer_store_b64(v, r, off, 0, SC1); }
DEV void st4s(rsrc_t r, u32 off, u32 v) { __builtin_amdgcn_raw_buffer_store_b32(v, r, off, 0, SC1); }
DEV void drain() { asm volatile("s_waitcnt vmcnt(0)" ::: "memory"); }
DEV void cbar() { asm volatile("" ::: "memory"); }
DEV void bar() { __syncthreads(); }
DEV int uni(int v) { return __builtin_amdgcn_readfirstlane(v); }

struct Ctl {
  GAS u32* c;
  u32 t_in;
  DEV void hold_until(u32 ticks) const { while ((u32)__builtin_amdgcn_s_memrealtime() - t_in < ticks) __builtin_amdgcn_s_sleep(8); asm volatile("" ::: "memory"); }
  bool dead;
  DEV GAS u32* line(int l) const { return c + l * 32; }
  DEV bool spin(u32& n) {
    if (++n > (1u << 17)) { __hip_atomic_store(line(L_ABORT), 1u, RLX_AGENT); dead = true; return true; }
    if ((n & 255u) == 0u && uni(__hip_atomic_load(line(L_ABORT), RLX_AGENT))) { dead = true; return true; }
    return false;
  }
  DEV u32 wait_ge(int l, u32 want) {
    u32 n = 0, v = want;
    if (dead) return want;
    for (;;) {
      v = uni(__hip_atomic_load(line(l), RLX_AGENT));
      if (v >= want) break;
      __builtin_amdgcn_s_sleep(2);
      if (spin(n)) { v = want; break; }
    }
    __builtin_amdgcn_fence(__ATOMIC_ACQUIRE, "wavefront");
    return v;
  }
  DEV void wait_lanes(int l, u32 want, u32& seen) {
    u32 n = 0;
    if (!__all(seen >= want) && !dead) {
      for (;;) {
        seen = __hip_atomic_load(line(l), RLX_AGENT);
        if (__all(seen >= want)) break;
        __builtin_amdgcn_s_sleep(2);
        if (spin(n)) break;
      }
    }
    __builtin_amdgcn_fence(__ATOMIC_ACQUIRE, "wavefront");
  }
  DEV void post(int l, u32 v) { __hip_atomic_store(line(l), v, RLX_AGENT); }
};

struct LnRow { float mean, rinv; };
DEV void ln_load(const u16* xrow, int c16, u32x4 (&v)[4]) {
#pragma unroll
  for (int i = 0; i < 4; ++i) v[i] = *(const u32x4*)(xrow + (c16 + 16 * i) * 8);
}
DEV LnRow ln_stats(const u32x4 (&v)[4]) {
  float s = 0.f;
#pragma unroll
  for (int i = 0; i < 4; ++i)
#pragma unroll
    for (int j = 0; j < 4; ++j) s += bflo(v[i][j]) + bfhi(v[i][j]);
  const float mean = row16_sum(s) * (1.f / 512.f);
  float q = 0.f;
#pragma unroll
  for (int i = 0; i < 4; ++i)
#pragma unroll
    for (int j = 0; j < 4; ++j) { float a = bflo(v[i][j]) - mean, b = bfhi(v[i][j]) - mean; q += a * a + b * b; }
  const float var = row16_sum(q) * (1.f / 512.f);
  const float sd = rbf(__builtin_sqrtf(rbf(rbf(var) + 1e-5f)));
  LnRow r; r.mean = rbf(mean); r.rinv = 1.0f / sd;
  return r;
}
DEV u32 ln_pair(u32 u, LnRow r) { u32 d = pk2(bflo(u) - r.mean, bfhi(u) - r.mean); return pk2(bflo(d) * r.rinv, bfhi(d) * r.rinv); }
DEV u32x4 ln_apply(u32x4 v, LnRow r) { u32x4 o; o[0] = ln_pair(v[0], r); o[1] = ln_pair(v[1], r); o[2] = ln_pair(v[2], r); o[3] = ln_pair(v[3], r); return o; }
DEV u32 xn_off(int row, int chunk) { return row * 1024 + ((chunk ^ (row & 15)) << 4); }

template <int MODE> DEV int wswz(int row) { return MODE == 0 ? ((row & 3) | (((row >> 3) & 1) << 2)) : ((row & 1) | (((row >> 2) & 3) << 1)); }
DEV void wraw_load(const u16* w, int h, int tid, u32x4 (&r)[16], int rot = 0) {
  const rsrc_t rs = mkrsrc(w, 4u << 20);
  const u32 vo = (u32)(tid >> 4) * 8192 + (tid & 15) * 16;
#pragma unroll
  for (int i = 0; i < 16; ++i) r[i] = __builtin_amdgcn_raw_buffer_load_b128(rs, vo, h * 256 + ((i + rot) & 15) * 262144, 0);
}
template <int MODE> DEV void wraw_store(ldsp L, int tid, const u32x4 (&r)[16], int rot = 0) {
  const int row = tid >> 4, c = tid & 15;
  const ldsp b = L + row * 256 + (((c >> 1) ^ wswz<MODE>(row)) << 5) + (c & 1) * 16;
#pragma unroll
  for (int i = 0; i < 16; ++i) *(LAS u32x4*)(b + ((i + rot) & 15) * 8192) = r[i];
}
DEV u32x4 wfrag_dense(ldsp L, int lane, int et, int ks) {
  const int g = lane >> 4, q = (lane >> 2) & 3, pp = lane & 3;
  const int r0 = 32 * ks + 8 * g + q;
  const u32 a = r0 * 256 + ((et ^ wswz<0>(r0)) << 5) + pp * 8;
  u32x2 lo = tr2(L + a), hi = tr2(L + a + 1024);
  return (u32x4){lo[0], lo[1], hi[0], hi[1]};
}

DEV void xn_fetch(Ctl& C, const unsigned char* xn, int s, int tid, ldsp dst, u32& seen) {
  C.wait_lanes(L_M1 + (tid & 31), (tid & 31) == (s >> 1) ? 1u : 0u, seen);
  const int w = uni(tid >> 6);
#pragma unroll
  for (int i = 0; i < 4; ++i)
    __builtin_amdgcn_global_load_lds((const GAS u32*)(xn + (size_t)s * 32768 + i * 8192 + tid * 16), (LAS u32*)(dst + i * 8192 + w * 1024), 16, 0, SC1);
}
DEV void ln_first(u32x4 (&a)[4]) {
  const LnRow sa = ln_stats(a);
#pragma unroll
  for (int i = 0; i < 4; ++i) { a[i] = ln_apply(a[i], sa); asm volatile("" : "+v"(a[i])); }
}
DEV void ln_first_put(ldsp L, int tid, const u32x4 (&a)[4]) {
#pragma unroll
  for (int i = 0; i < 4; ++i) *(LAS u32x4*)(L + xn_off(tid >> 4, (tid & 15) + 16 * i)) = a[i];
}
DEV u32x4 proj_kfmt(ldsp XN, const u32x4 (&wf)[32], int lane, int rt) {
  f32x4 a0 = {0, 0, 0, 0}, a1 = {0, 0, 0, 0};
  asm volatile("" : "+v"(lane));
  const int row = 16 * rt + (lane & 15), g = lane >> 4;
  ldsp b[4];
#pragma unroll
  for (int k = 0; k < 4; ++k) b[k] = XN + xn_off(row, 4 * k + g);
  u32x4 xf[4];
#pragma unroll
  for (int k = 0; k < 4; ++k) xf[k] = lds16(b[k]);
#pragma unroll
  for (int ks = 0; ks < 16; ++ks) {
    __builtin_amdgcn_sched_barrier(0);
    a0 = mfma32(wf[ks], xf[ks & 3], a0); a1 = mfma32(wf[16 + ks], xf[ks & 3], a1);
    __builtin_amdgcn_sched_barrier(0);
    if (ks + 4 < 16) xf[ks & 3] = lds16(b[ks & 3] + ((ks >> 2) + 1) * 256);
  }
  return (u32x4){pk2(a0[0], a0[1]), pk2(a0[2], a0[3]), pk2(a1[0], a1[1]), pk2(a1[2], a1[3])};
}

DEV void role_k(const Params& P, Ctl& C, ldsp L, int h) {
  const int tid = threadIdx.x, lane = tid & 63, w = uni(tid >> 6);
  u32x4 wf[32];
  {
    u32x4 r[16], xa[4];
    ln_load(P.x + (size_t)(tid >> 4) * DM, tid & 15, xa);
    ln_first(xa);
    wraw_load(P.wk, h, tid, r);
    wraw_store<0>(L, tid, r);
    bar();
#pragma unroll
    for (int ks = 0; ks < 16; ++ks) { wf[ks] = wfrag_dense(L, lane, 2 * (w >> 1), ks); wf[16 + ks] = wfrag_dense(L, lane, 2 * (w >> 1) + 1, ks); }
    bar();
    ln_first_put(L, tid, xa);
  }
  const rsrc_t dst = mkrsrc(P.ws + OFF_KX, 16u << 20);
  u32 seenX = 0;
  for (int s = 0; s < NSEG; ++s) {
    ldsp XN = L + (s & 1) * 32768;
    drain();
    bar();
    if (tid == 0 && s > 0) C.post(L_KV + h, s);
    if (s + 1 < NSEG) xn_fetch(C, P.ws + OFF_XN, s + 1, tid, L + ((s + 1) & 1) * 32768, seenX);
    const u32x4 o = proj_kfmt(XN, wf, lane, w & 1);
    st16s(dst, (u32)(s * 32 + h) * 8192 + ((w & 1) * 4 + (w >> 1)) * 1024 + lane * 16, o);
  }
  drain();
  bar();
  if (tid == 0) C.post(L_KV + h, NSEG);
}

DEV void role_v(const Params& P, Ctl& C, ldsp L, int h) {
  const int tid = threadIdx.x, lane = tid & 63, w = uni(tid >> 6), g = lane >> 4;
  u32x4 wf[32], wo[16];
  {
    u32x4 r[16], xa[4];
    {
      u32x4 r0[16];
      ln_load(P.x + (size_t)(tid >> 4) * DM, tid & 15, xa);
      wraw_load(P.wv, h, tid, r0);
      const rsrc_t rs = mkrsrc(P.wo, 4u << 20);
#pragma unroll
      for (int i = 0; i < 16; ++i) r[i] = __builtin_amdgcn_raw_buffer_load_b128(rs, (u32)(tid >> 4) * 1024 + (tid & 15) * 16, h * 131072 + (i & 3) * 32768 + (i >> 2) * 256, 0);
      ln_first(xa);
      wraw_store<0>(L, tid, r0);
    }
    bar();
#pragma unroll
    for (int ks = 0; ks < 16; ++ks) { wf[ks] = wfrag_dense(L, lane, 2 * (w >> 1), ks); wf[16 + ks] = wfrag_dense(L, lane, 2 * (w >> 1) + 1, ks); }
    bar();
    wraw_store<0>(L, tid, r);
    bar();
#pragma unroll
    for (int i = 0; i < 16; ++i) {
      const int et = (4 * w + (i >> 2)) & 7, r0 = (w >> 1) * 128 + (i & 3) * 32 + 4 * g + ((lane >> 2) & 3), r1 = r0 + 16;
      const u32x2 lo = tr2(L + r0 * 256 + ((et ^ wswz<0>(r0)) << 5) + (lane & 3) * 8), hi = tr2(L + r1 * 256 + ((et ^ wswz<0>(r1)) << 5) + (lane & 3) * 8);
      wo[i] = (u32x4){lo[0], lo[1], hi[0], hi[1]};
    }
    bar();
    ln_first_put(L, tid, xa);
  }
  const rsrc_t dv = mkrsrc(P.ws + OFF_VX, 16u << 20), dw = mkrsrc(P.ws + OFF_VW, 64u << 20);
  ldsp VT = L + 65536;
  const u32 a_vt = (u32)(w >> 1) * 1024 + ((lane & 3) * 16 + 4 * g + ((lane >> 2) & 3)) * 16 + (w & 1) * 8;
  u32 seenX = 0;
  bar();
  for (int s = 0; s < NSEG; ++s) {
    ldsp XN = L + (s & 1) * 32768;
    if (s + 1 < NSEG) xn_fetch(C, P.ws + OFF_XN, s + 1, tid, L + ((s + 1) & 1) * 32768, seenX);
    const u32x4 vk = proj_kfmt(XN, wf, lane, w & 1);
    VT = L + 65536 + (s & 1) * 8192;
    *(LAS u32x4*)(VT + ((w & 1) * 4 + (w >> 1)) * 1024 + lane * 16) = vk;
    drain();
    bar();
    if (tid == 0 && s > 5) C.post(L_KV + 32 + h, s);
    {
      const u32x2 lo = tr2(VT + a_vt), hi = tr2(VT + 4096 + a_vt);
      st16s(dv, (u32)(s * 32 + h) * 8192 + w * 1024 + lane * 16, (u32x4){lo[0], lo[1], hi[0], hi[1]});
    }
#pragma unroll
    for (int hf = 0; hf < 2; ++hf) {
      f32x4 c[4];
#pragma unroll
      for (int i = 0; i < 4; ++i) c[i] = (f32x4){0, 0, 0, 0};
#pragma unroll
      for (int ks = 0; ks < 4; ++ks) {
        const u32x4 f0 = lds16(VT + ks * 1024 + lane * 16), f1 = lds16(VT + 4096 + ks * 1024 + lane * 16);
#pragma unroll
        for (int c2 = 0; c2 < 2; ++c2) { c[2 * c2] = mfma32(f0, wo[(hf * 2 + c2) * 4 + ks], c[2 * c2]); c[2 * c2 + 1] = mfma32(f1, wo[(hf * 2 + c2) * 4 + ks], c[2 * c2 + 1]); }
      }
#pragma unroll
      for (int c2 = 0; c2 < 2; ++c2)
        st16s(dw, (u32)(s * 32 + h) * 32768 + (4 * w + hf * 2 + c2) * 1024 + lane * 16,
              (u32x4){pk2(c[2 * c2][0], c[2 * c2][1]), pk2(c[2 * c2][2], c[2 * c2][3]), pk2(c[2 * c2 + 1][0], c[2 * c2 + 1][1]), pk2(c[2 * c2 + 1][2], c[2 * c2 + 1][3])});
      __builtin_amdgcn_sched_barrier(0);
    }
    if (s < 5) {
      drain();
      bar();
      if (tid == 0) C.post(L_KV + 32 + h, s + 1);
    }
  }
  drain();
  bar();
  if (tid == 0) C.post(L_KV + 32 + h, NSEG);
}

template <int N> DEV u32 umax_ror(u32 v) { const u32 o = (u32)__builtin_amdgcn_update_dpp(0, (int)v, 0x120 + N, 0xf, 0xf, false); return o > v ? o : v; }
DEV u32 wave_max_u32(u32 v) {
  v = umax_ror<8>(v); v = umax_ror<4>(v); v = umax_ror<2>(v); v = umax_ror<1>(v);
  { auto r = __builtin_amdgcn_permlane16_swap(v, v, false, false); v = r[0] > r[1] ? r[0] : r[1]; }
  { auto r = __builtin_amdgcn_permlane32_swap(v, v, false, false); v = r[0] > r[1] ? r[0] : r[1]; }
  return v;
}
DEV void role_router(const Params& P, Ctl& C, ldsp L, int e) {
  const int tid = threadIdx.x;
  const rsrc_t rmoe = mkrsrc(P.ws + OFF_MOE, 65536), rxn = mkrsrc(P.ws + OFF_XN, 2u << 20);
  {
    u32x4 wr[4], xh[2][4];
    ln_load(P.x + (size_t)(e * 64 + (tid >> 4)) * DM, tid & 15, xh[0]);
    ln_load(P.x + (size_t)(e * 64 + 32 + (tid >> 4)) * DM, tid & 15, xh[1]);
    ln_load(P.wr, tid & 15, wr);
#pragma unroll
    for (int half = 0; half < 2; ++half) {
      const u32x4 (&xv)[4] = xh[half];
      const LnRow st = ln_stats(xv);
      float d = 0.f;
#pragma unroll
      for (int i = 0; i < 4; ++i) { const u32x4 y = ln_apply(xv[i], st);
        st16s(rxn, (e * 2 + half) * 32768 + xn_off(tid >> 4, (tid & 15) + 16 * i), y);
#pragma unroll
        for (int j = 0; j < 4; ++j) d += bflo(y[j]) * bflo(wr[i][j]) + bfhi(y[j]) * bfhi(wr[i][j]); }
      d = row16_sum(d);
      if ((tid & 15) == 0) ((LAS u16*)L)[half * 32 + (tid >> 4)] = (u16)(pk2(d, d) & 0xffffu);
    }
    drain();
    bar();
    if (tid < 32) { st4s(rmoe, MOE_R + e * 128 + tid * 4, ((LAS u32*)L)[tid]); drain(); if (tid == 0) C.post(L_M1 + e, 1); }
  }
}
DEV void role_ffn(const Params& P, Ctl& C, ldsp L, int e) {
  const int tid = threadIdx.x, lane = tid & 63, w = uni(tid >> 6), g = lane >> 4, n = lane & 15, q = (lane >> 2) & 3, pp = lane & 3;
  const rsrc_t rmoe = mkrsrc(P.ws + OFF_MOE, 65536);
  u32x4 rd[7];
  {
    const int row = lane >> 3, cp = lane & 7;
    const u16* src = (w < 4 ? P.wg : P.wu) + (size_t)((w & 3) * 128 + row) * FF + (e < 28 ? e : 0) * 64 + ((((cp >> 1) ^ (row & 3)) << 1) + (cp & 1)) * 8;
    for (int i = 0; i < 16; ++i)
      __builtin_amdgcn_global_load_lds((const GAS u32*)(src + (size_t)i * 8 * FF), (LAS u32*)(L + w * 16384 + i * 1024), 16, 0, 0);
    const rsrc_t g2 = mkrsrc(P.wd, FF * DM * 2);
#pragma unroll
    for (int i = 0; i < 7; ++i) rd[i] = __builtin_amdgcn_raw_buffer_load_b128(g2, (u32)(tid >> 1) * 1024 + (tid & 1) * 16 + e * 32, i * 262144, 0);
  }
  const ldsp LG = L;
  L += 131072;
  LAS u32* CAND = (LAS u32*)(L + 1024);
  LAS u32* TOP = (LAS u32*)(L + 2048);
  ldsp XS = L + 4096;
  { u32 sn = 0; C.wait_lanes(L_M1 + w * 4 + (lane & 3), 1, sn); }
  {
    const u32x2 rv = ld8s(rmoe, MOE_R + tid * 8);
    u32 key[4];
#pragma unroll
    for (int i = 0; i < 4; ++i) {
      u32 b = (i & 1) ? (rv[i >> 1] >> 16) : (rv[i >> 1] & 0xffffu);
      if (b == 0x8000u) b = 0;
      const u32 ord = (b & 0x8000u) ? (~b & 0xffffu) : (b | 0x8000u);
      key[i] = (ord << 16) | (u32)(0xffff - (tid * 4 + i));
    }
    for (int it = 0; it < 8; ++it) {
      u32 m01 = key[0] > key[1] ? key[0] : key[1], m23 = key[2] > key[3] ? key[2] : key[3];
      const u32 mx = wave_max_u32(m01 > m23 ? m01 : m23);
#pragma unroll
      for (int i = 0; i < 4; ++i) key[i] = key[i] == mx ? 0u : key[i];
      if (lane == 0) CAND[w * 8 + it] = mx;
    }
    bar();
    u32 c = CAND[lane];
    for (int it = 0; it < 8; ++it) {
      const u32 mx = wave_max_u32(c);
      c = c == mx ? 0u : c;
      if (tid == 0) TOP[it] = mx;
    }
    bar();
  }
  {
    const int r = tid >> 4;
    if (r < 16) {
      u32x4 xv[4];
      const int row = 0xffff - (int)(TOP[r & 7] & 0xffffu);
      ln_load(P.x + (size_t)row * DM, tid & 15, xv);
      const LnRow st = ln_stats(xv);
#pragma unroll
      for (int i = 0; i < 4; ++i) { u32x4 y = ln_apply(xv[i], st); if (r >= 8) y = (u32x4){0, 0, 0, 0}; *(LAS u32x4*)(XS + xn_off(r, (tid & 15) + 16 * i)) = y; }
    }
    drain();
    bar();
  }
  LAS float* GU = (LAS float*)(L + 4096 + 16384);
  LAS u16* HT = (LAS u16*)(L + 4096 + 16384 + 4096);
  if (e < 28) {
    f32x4 a = {0, 0, 0, 0};
#pragma unroll
    for (int ks = 0; ks < 16; ++ks) {
      const u32 a0 = (u32)(w & 1) * 65536 + (32 * ks + 8 * g + q) * 128 + (((w >> 1) ^ q) << 5) + pp * 8;
      const u32x2 lo = tr2(LG + a0), hi = tr2(LG + a0 + 512);
      a = mfma32(lds16(XS + xn_off(n, 4 * ks + g)), (u32x4){lo[0], lo[1], hi[0], hi[1]}, a);
    }
    if (g < 2) {
#pragma unroll
      for (int r = 0; r < 4; ++r) GU[(w * 8 + 4 * g + r) * 16 + n] = a[r];
    }
    bar();
    {
      const int t = tid >> 6, f = tid & 63;
      const float gv = rbf(GU[((f >> 4) * 16 + t) * 16 + (f & 15)]), uv = rbf(GU[((f >> 4) * 16 + 8 + t) * 16 + (f & 15)]);
      const float sl = rbf(gv / (1.0f + __expf(-gv)));
      HT[t * 64 + f] = (u16)(pk2(sl * uv, 0.f) & 0xffffu);
    }
    bar();
    if (tid < 64) { st16s(rmoe, MOE_H + (tid >> 3) * (FF * 2) + e * 128 + (tid & 7) * 16, lds16((ldsp)HT + tid * 16)); drain(); if (tid == 0) C.post(L_M2 + e, 1); }
  }
  {
    u32x4 wd[7];
    bar();
#pragma unroll
    for (int i = 0; i < 7; ++i) *(LAS u32x4*)(LG + i * 8192 + tid * 16) = rd[i];
    bar();
#pragma unroll
    for (int ks = 0; ks < 7; ++ks) {
      const u32 a0 = (u32)(w * 224 + 32 * ks + 8 * g + q) * 32 + pp * 8;
      const u32x2 lo = tr2(LG + a0), hi = tr2(LG + a0 + 128);
      wd[ks] = (u32x4){lo[0], lo[1], hi[0], hi[1]};
    }
    const int w0 = (w * 224) >> 6, w1 = (w * 224 + 223) >> 6;
    { u32 sn = 0; const int i = w0 + (lane & 7); C.wait_lanes(L_M2 + (i < w1 ? i : w1), 1, sn); }
    f32x4 a = {0, 0, 0, 0};
    u32x4 hf[7];
#pragma unroll
    for (int ks = 0; ks < 7; ++ks) hf[ks] = ld16s(rmoe, MOE_H + (n & 7) * (FF * 2) + (w * 224 + ks * 32 + 8 * g) * 2);
#pragma unroll
    for (int ks = 0; ks < 7; ++ks) a = mfma32(hf[ks], wd[ks], a);
    bar();
    if (g < 2) {
#pragma unroll
      for (int r = 0; r < 4; ++r) GU[(w * 8 + 4 * g + r) * 16 + n] = a[r];
    }
    bar();
    if (tid < 64) {
      const int t = tid >> 3, cp = tid & 7;
      float s0 = 0.f, s1 = 0.f;
#pragma unroll
      for (int i = 0; i < 8; ++i) { s0 += GU[(i * 8 + t) * 16 + 2 * cp]; s1 += GU[(i * 8 + t) * 16 + 2 * cp + 1]; }
      const u32 key = TOP[t];
      const u32 ord = key >> 16, bits = (ord & 0x8000u) ? (ord & 0x7fffu) : (~ord & 0xffffu);
      const float rs = rbf(1.0f / (1.0f + __expf(-bflo(bits))));
      st4s(rmoe, MOE_U + t * 1024 + e * 32 + cp * 4, pk2(rbf(s0) * rs, rbf(s1) * rs));
      if (tid < 8) st4s(rmoe, MOE_I + e * 32 + tid * 4, 0xffffu - (TOP[tid] & 0xffffu));
      drain();
      if (tid == 0) C.post(L_M3 + e, 1);
    }
  }
}

DEV void role_probe(Ctl& C, unsigned char* xch, int h, bool far) {
  const int tid = threadIdx.x, lane = tid & 63, w = uni(tid >> 6);
  if (w < 6) {
    const int pi = h * 6 + w;
    const rsrc_t rx = mkrsrc(xch + (size_t)pi * 16384, 16384);
    u32 b0 = ~0u, b1 = ~0u;
#pragma unroll
    for (int it = 0; it < 6; ++it) {
      const u32 off = (it & 1) * 8192 + (it >> 1) * 1024 + (lane & 31) * 4;
      cbar();
      const u64 t0 = __builtin_amdgcn_s_memtime();
      __builtin_amdgcn_s_waitcnt(0xC07F);
      st4s(rx, off, SENT);
      drain();
      const u64 t1 = __builtin_amdgcn_s_memtime();
      __builtin_amdgcn_s_waitcnt(0xC07F);
      const u32 dt = (u32)(t1 - t0);
      if (it & 1) b1 = dt < b1 ? dt : b1; else b0 = dt < b0 ? dt : b0;
    }
    const u32 sel = uni(((b1 < b0) != far) ? 1 : 0);
    const u32x4 z = {SENT, SENT, SENT, SENT};
#pragma unroll
    for (int i = 0; i < 8; ++i) st16s(rx, sel * 8192 + i * 1024 + lane * 16, z);
    drain();
    if (lane == 0) __hip_atomic_store(C.line(L_TBL) + pi, sel + 1u, RLX_AGENT);
  }
}
DEV void role_attn(const Params& P, Ctl& C, ldsp L, int h, bool helper) {
  const int tid = threadIdx.x, lane = tid & 63, w = uni(tid >> 6);
  C.hold_until(HOLD);
  bar();
  u32x4 wf[32];
  {
    u32x4 r[16];
    wraw_load(P.wq, h, tid, r);
    wraw_store<0>(L, tid, r);
    bar();
#pragma unroll
    for (int ks = 0; ks < 16; ++ks) { wf[ks] = wfrag_dense(L, lane, 2 * (w >> 1), ks); wf[16 + ks] = wfrag_dense(L, lane, 2 * (w >> 1) + 1, ks); }
    bar();
  }
  const rsrc_t rk = mkrsrc(P.ws + OFF_KX, 16u << 20), rv = mkrsrc(P.ws + OFF_VX, 16u << 20), ro = mkrsrc(P.ws + OFF_O, 16u << 20),
               rm = mkrsrc(P.ws + OFF_KM, 4u << 20), rp = mkrsrc(P.ws + OFF_PM, 1u << 20);
  ldsp QL = L + 65536, KL = L + 65536 + 8192;
  const int rt = w & 1, q4 = w >> 1, g = lane >> 4, tl = lane & 15;
  u32 seenKV = 0, seenM = 0, seenX = 0, fm = 0;
  u32x4 kx, vn0, vn1, kn[4];
  bool earlyn = false;
  auto fetch = [&](int sg) __attribute__((always_inline)) {
    const u32 base = (u32)(sg * 32 + h) * 8192;
    C.wait_lanes(L_KV + (lane & 1) * 32 + h, sg + 1, seenKV);
    kx = ld16s(rk, base + tid * 16);
    vn0 = ld16s(rv, base + (2 * q4) * 1024 + lane * 16); vn1 = ld16s(rv, base + (2 * q4 + 1) * 1024 + lane * 16);
    earlyn = seenM >= (u32)sg + 1;
    if (earlyn) {
#pragma unroll
      for (int p = 0; p < 4; ++p) kn[p] = ld16s(rm, (u32)(sg * 32 + h) * 2048 + p * 512 + g * 128 + (tl & 7) * 16);
    }
    fm = 0;
    if (seenM < (u32)sg + 3) fm = __hip_atomic_load(C.line(L_CH + 4 * h), RLX_AGENT);
  };
  auto next = [&](int sg) { return sg + (sg < S_HELPA ? 1 : 2); };
  const int s0 = helper ? S_HELPA + 1 : 0, lo = (helper ? L_O2 : L_O) + h;
  xn_fetch(C, P.ws + OFF_XN, s0, tid, L, seenX);
  xn_fetch(C, P.ws + OFF_XN, next(s0), tid, L + 32768, seenX);
  fetch(s0);
  drain();
  bar();
  int prev = -1;
  for (int s = s0, it = 0; s < NSEG; prev = s, s = next(s), ++it) {
    const int sn = next(s), snn = next(sn);
    ldsp XN = L + (it & 1) * 32768;
    QL = L + 65536 + (it & 1) * 16384; KL = QL + 8192;
    const u32 base = (u32)(s * 32 + h) * 8192;
    const u32x4 qf = proj_kfmt(XN, wf, lane, rt);
    *(LAS u32x4*)(QL + (rt * 4 + q4) * 1024 + lane * 16) = qf;
    *(LAS u32x4*)(KL + tid * 16) = kx;
    drain();
    bar();
    if (tid == 0 && prev >= 0 && prev < S_HELPA) C.post(lo, prev + 1);
    { const u32 f = uni(fm); seenM = f > seenM ? f : seenM; }
    const u32x4 vx0 = vn0, vx1 = vn1;
    u32x4 km[4] = {kn[0], kn[1], kn[2], kn[3]};
    const bool early = earlyn;
    if (!early) {
      if (seenM < (u32)s + 1) seenM = C.wait_ge(L_CH + 4 * h, s + 1);
      __builtin_amdgcn_fence(__ATOMIC_ACQUIRE, "wavefront");
#pragma unroll
      for (int p = 0; p < 4; ++p) km[p] = ld16s(rm, (u32)(s * 32 + h) * 2048 + p * 512 + g * 128 + (tl & 7) * 16);
    }
    if (snn < NSEG) xn_fetch(C, P.ws + OFF_XN, snn, tid, XN, seenX);
    if (sn < NSEG) fetch(sn);
    f32x4 sc[3];
    u32x4 qq[4];
#pragma unroll
    for (int kt = 0; kt < 3; ++kt) sc[kt] = (f32x4){0, 0, 0, 0};
#pragma unroll
    for (int p = 0; p < 4; ++p) {
      qq[p] = lds16(QL + (rt * 4 + p) * 1024 + lane * 16);
      sc[1] = mfma32(lds16(KL + p * 1024 + lane * 16), qq[p], sc[1]);
      sc[2] = mfma32(lds16(KL + (4 + p) * 1024 + lane * 16), qq[p], sc[2]);
    }
#pragma unroll
    for (int p = 0; p < 4; ++p) sc[0] = mfma32(km[p], qq[p], sc[0]);
    const float cs = 0.08838834764831845f * 1.4426950408889634f;
    const int tq = 16 * rt + tl;
    float mx = -3.0e38f;
#pragma unroll
    for (int kt = 0; kt < 3; ++kt)
#pragma unroll
      for (int r = 0; r < 4; ++r) {
        const bool ok = kt == 0 ? (g < 2) : (16 * (kt - 1) + 4 * g + r <= tq);
        const float v = ok ? sc[kt][r] : -__builtin_inff();
        sc[kt][r] = v; mx = fmaxf(mx, v);
      }
    mx = xmax_g(mx) * cs;
    float sum = 0.f;
#pragma unroll
    for (int kt = 0; kt < 3; ++kt)
#pragma unroll
      for (int r = 0; r < 4; ++r) { const float e = __builtin_amdgcn_exp2f(__builtin_fmaf(sc[kt][r], cs, -mx)); sc[kt][r] = e; sum += e; }
    sum = xsum_g(sum);
    const float inv = __builtin_amdgcn_rcpf(sum);
    const u32x2 pm = {pk2(sc[0][0] * inv, sc[0][1] * inv), pk2(sc[0][2] * inv, sc[0][3] * inv)};
    const u32x4 px = {pk2(sc[1][0] * inv, sc[1][1] * inv), pk2(sc[1][2] * inv, sc[1][3] * inv), pk2(sc[2][0] * inv, sc[2][1] * inv), pk2(sc[2][2] * inv, sc[2][3] * inv)};
    if (q4 == 0 && g < 2) st8s(rp, (u32)(s * 32 + h) * 512 + tq * 16 + g * 8, pm);
    const f32x4 z = {0, 0, 0, 0};
    const f32x4 o0 = mfma32(vx0, px, z), o1 = mfma32(vx1, px, z);
    const u32x4 of = {pk2(o0[0], o0[1]), pk2(o0[2], o0[3]), pk2(o1[0], o1[1]), pk2(o1[2], o1[3])};
    st16s(ro, base + (rt * 4 + q4) * 1024 + lane * 16, of);
    if (s >= S_HELPA) {
      drain();
      bar();
      if (tid == 0) C.post(lo, s + 1);
    }
  }
}

constexpr int S_HELP = 40;
DEV void role_out(const Params& P, Ctl& C, ldsp L, int ct, int par, bool helper) {
  const int tid = threadIdx.x, lane = tid & 63, w = uni(tid >> 6), g = lane >> 4, tl = lane & 15;
  u32x4 wf[32];
  C.hold_until(HOLD);
  bar();
#pragma unroll
  for (int hf = 0; hf < 2; ++hf) {
    u32x4 r[16];
    const rsrc_t rs = mkrsrc(P.wo, 4u << 20);
#pragma unroll
    for (int i = 0; i < 16; ++i)
      r[i] = __builtin_amdgcn_raw_buffer_load_b128(rs, (u32)(tid >> 2) * 1024 + (tid & 3) * 16, ((i >> 1) * 4 + ((2 * hf + (i & 1) + ct) & 3)) * 131072 + ct * 64, 0);
#pragma unroll
    for (int i = 0; i < 16; ++i) *(LAS u32x4*)(L + i * 8192 + tid * 16) = r[i];
    bar();
#pragma unroll
    for (int k8 = 0; k8 < 8; ++k8) {
      const u32 a = (u32)(w * 256 + k8 * 32 + 4 * g + ((lane >> 2) & 3)) * 64 + (lane & 3) * 8;
      const u32x2 l0 = tr2(L + a), h0_ = tr2(L + a + 1024), l1 = tr2(L + a + 32), h1_ = tr2(L + a + 1056);
      wf[hf * 8 + k8] = (u32x4){l0[0], l0[1], h0_[0], h0_[1]}; wf[16 + hf * 8 + k8] = (u32x4){l1[0], l1[1], h1_[0], h1_[1]};
    }
    bar();
  }
  const rsrc_t ro = mkrsrc(P.ws + OFF_O, 16u << 20), rmoe = mkrsrc(P.ws + OFF_MOE, 65536), rxn = mkrsrc(P.ws + OFF_XN, 2u << 20),
               rp = mkrsrc(P.ws + OFF_PM, 1u << 20), rw = mkrsrc(P.ws + OFF_VWM, 16u << 20);
  LAS float* RED = (LAS float*)L;
  LAS u32* UP = (LAS u32*)(L + 32768);
  LAS int* IDX = (LAS int*)(L + 32768 + 1024);
  if (w == 0) {
    C.wait_ge(L_M3 + 2 * ct, 1); C.wait_ge(L_M3 + 2 * ct + 1, 1);
    UP[lane] = ld4s(rmoe, MOE_U + (lane >> 4) * 1024 + ct * 64 + tl * 4);
    UP[64 + lane] = ld4s(rmoe, MOE_U + (4 + (lane >> 4)) * 1024 + ct * 64 + tl * 4);
    if (lane < 8) IDX[lane] = (int)ld4s(rmoe, MOE_I + ct * 64 + lane * 4);
    if (lane < 16) UP[128 + lane] = 0;
  }
  bar();
  u64 segmask = 0;
#pragma unroll
  for (int i = 0; i < 8; ++i) segmask |= 1ull << ((uni(IDX[i]) >> 5) & 63);
  int it = 0;
  u32 seen = 0;
  bool ahead = false;
  u32x4 of[16];
  const u32 hq[4] = {(u32)((0 + ct) & 3) * 8192, (u32)((1 + ct) & 3) * 8192, (u32)((2 + ct) & 3) * 8192, (u32)((3 + ct) & 3) * 8192};
  const int fk = (lane >> 2) & 3;
  const int fl = fk == 1 ? L_CH + (4 * w + (lane & 3)) * 4 + (ct >> 2) : (fk == 2 ? L_O2 : L_O) + 4 * w + (lane & 3);
  auto want = [&](int sg) -> u32 { const bool hs = sg > S_HELPA && ((sg - S_HELPA) & 1); return fk == 1 ? sg + 2 : ((fk == 2) == hs ? sg + 1 : 0); };
  const int t = (tid >> 4) & 15, cp = tid & 15;
  for (int s = helper ? S_HELP + 1 : 0, sn; s < NSEG; s = sn) {
    sn = s + (s < S_HELP ? 1 : 2);
    const int row = s * 32 + par * 16 + t;
    const u32 xx = *(const u32*)(P.x + (size_t)row * DM + ct * 32 + cp * 2);
    const u32 xn = ld4s(rxn, s * 32768 + xn_off(par * 16 + t, ct * 4 + (cp >> 2)) + (cp & 3) * 4);
    f32x4 a0 = {0, 0, 0, 0}, a1 = {0, 0, 0, 0};
    const u32 base = (u32)(s * 32 + 4 * w) * 8192 + par * 4096;
    if (!ahead) {
      C.wait_lanes(fl, want(s), seen);
#pragma unroll
      for (int j = 0; j < 16; ++j) of[j] = ld16o(ro, lane * 16, base + hq[j >> 2] + (j & 3) * 1024);
    }
    const u32 hb = (u32)(s * 32 + 4 * w + g);
    const u32x4 m0 = ld16s(rw, hb * 8192 + (ct * 32 + tl) * 16), m1 = ld16s(rw, hb * 8192 + (ct * 32 + 16 + tl) * 16);
    const u32x4 m2 = ld16s(rp, hb * 512 + par * 256 + tl * 16);
    ahead = sn < NSEG && __all(seen >= want(sn));
    const u32 nbase = (u32)(sn * 32 + 4 * w) * 8192 + par * 4096;
#pragma unroll
    for (int j = 0; j < 16; ++j) {
      __builtin_amdgcn_sched_barrier(0);
      a0 = mfma32(wf[j], of[j], a0); a1 = mfma32(wf[16 + j], of[j], a1);
      __builtin_amdgcn_sched_barrier(0);
      if (ahead) of[j] = ld16o(ro, lane * 16, nbase + hq[j >> 2] + (j & 3) * 1024);
    }
    a0 = mfma32(m0, m2, a0); a1 = mfma32(m1, m2, a1);
    if (!ahead && sn < NSEG) seen = __hip_atomic_load(C.line(fl), RLX_AGENT);
    LAS float* R = RED + (it & 1) * 4096;
    *(LAS f32x4*)(R + ((w * 2 + 0) * 64 + lane) * 4) = a0; *(LAS f32x4*)(R + ((w * 2 + 1) * 64 + lane) * 4) = a1;
    ++it;
    bar();
    if (tid < 256) {
      const int src = ((cp >> 3) * 64 + ((cp >> 1) & 3) * 16 + t) * 4 + (cp & 1) * 2;
      float s0 = 0.f, s1 = 0.f;
#pragma unroll
      for (int i = 0; i < 8; ++i) { const f32x2 v = *(LAS f32x2*)(R + i * 512 + src); s0 += v[0]; s1 += v[1]; }
      u32 up = 0;
      if ((segmask >> s) & 1) {
        int mi = 8;
#pragma unroll
        for (int i = 0; i < 8; ++i) mi = IDX[i] == row ? i : mi;
        up = UP[mi * 16 + cp];
      }
      const u32 zz = pk2(bflo(xn) * 0.0f + bflo(up), bfhi(xn) * 0.0f + bfhi(up));
      const u32 bb = pk2(bflo(xn) + bflo(zz), bfhi(xn) + bfhi(zz));
      const u32 aa = pk2(s0, s1);
      const u32 xa = pk2(bflo(xx) + bflo(aa), bfhi(xx) + bfhi(aa));
      *(u32*)(P.out + (size_t)row * DM + ct * 32 + cp * 2) = pk2(bflo(xa) + bflo(bb), bfhi(xa) + bfhi(bb));
    }
  }
}

constexpr int C_MEM = 0, C_KF = 8192, C_KX = 12288, C_VX = 24576, C_QF = 32768, C_P = C_QF + 2048, C_ST = C_P + 1536,
              C_VP = C_ST + 1024, C_TS = C_VP + 2048, C_W = 40960, C_RAW = 32768;
static_assert(C_TS + 256 <= C_W && C_W + 98304 <= LDS_BYTES && C_RAW + 131072 <= LDS_BYTES, "lds");

DEV int sp_row(int c4, int gb, int q) { return 16 * (2 * (c4 >> 1) + (gb >> 1)) + 8 * (q >> 1) + 2 * (2 * (gb & 1) + (c4 & 1)) + (q & 1); }
DEV int opq(int v) { asm volatile("" : "+v"(v)); return v; }
DEV void sched() { __builtin_amdgcn_sched_barrier(0); }
DEV f32x4 zero4() { f32x2 a, b; asm volatile("v_mov_b64 %0, 0\n\tv_mov_b64 %1, 0\n\ts_nop 1" : "=v"(a), "=v"(b)); return (f32x4){a[0], a[1], b[0], b[1]}; }
DEV u32 vconst(u32 c) { u32 r; asm volatile("v_mov_b32 %0, %1" : "=v"(r) : "s"(c)); return r; }
DEV void pin(f32x4& v) { asm volatile("" : "+v"(v)); }
DEV u32x8 wfrag_sparse(ldsp L, const u32 (&ad)[4]) {
  u32x8 f;
#pragma unroll
  for (int c4 = 0; c4 < 4; ++c4) { const u32x2 v = tr2(L + ad[c4]); f[2 * c4] = v[0]; f[2 * c4 + 1] = v[1]; }
  return f;
}
DEV u32x8 lds32(ldsp p) { const u32x4 a = lds16(p), b = lds16(p + 1024); return (u32x8){a[0], a[1], a[2], a[3], b[0], b[1], b[2], b[3]}; }
DEV void lds32w(ldsp p, u32x8 f) { *(LAS u32x4*)p = (u32x4){f[0], f[1], f[2], f[3]}; *(LAS u32x4*)(p + 1024) = (u32x4){f[4], f[5], f[6], f[7]}; }
DEV u32 half_sum_pk(f32x4 d) { float a = d[0], b = d[2], c = d[1], e = d[3]; swap32(a, b); swap32(c, e); return pk2(a + b, c + e); }
DEV u32x4 sent4() { u32 z = SENT; asm volatile("" : "+v"(z)); return (u32x4){z, z, z, z}; }
DEV bool all_set(u32x4 v) { const u32 a = v[0] < v[1] ? v[1] : v[0], b = v[2] < v[3] ? v[3] : v[2]; return __all((a < b ? b : a) != SENT); }
#define vmax(a, b) __builtin_amdgcn_fmed3f((a), (b), pinf)

DEV void role_chain(const Params& P, Ctl& C, unsigned char* xch, int id) {
  const ldsp L = reinterpret_cast<ldsp>(static_cast<uintptr_t>(C_MEM));
  float pinf = __builtin_inff(); asm volatile("" : "+s"(pinf));
  const int tid = threadIdx.x, lane = tid & 63, w = uni(tid >> 6), h = id >> 2, c4w = id & 3;
  u32x8 bq[8], bk[8], bw[8];
  u32x4 mv, mw;
  {
    u32x4 r0[16], r1[16];
    {
      u32x4 r2[16], r3[4];
      wraw_load(P.wv, h, tid, r2, 4 * c4w);
      const rsrc_t rs = mkrsrc(P.wo, 4u << 20);
#pragma unroll
      for (int i = 0; i < 4; ++i) r3[i] = __builtin_amdgcn_raw_buffer_load_b128(rs, (u32)(tid >> 4) * 1024 + (tid & 15) * 16, h * 131072 + i * 32768 + c4w * 256, 0);
#pragma unroll
      for (int i = 0; i < 16; ++i) { const int row = tid >> 4; *(LAS u32x4*)(L + ((i + 4 * c4w) & 15) * 8192 + row * 256 + (((tid & 15) ^ ((row & 1) | ((row >> 1) & 14))) << 4)) = r2[i]; }
#pragma unroll
      for (int i = 0; i < 4; ++i) { const int row = i * 32 + (tid >> 4), c = tid & 15; *(LAS u32x4*)(L + 131072 + row * 256 + (((c >> 1) ^ wswz<0>(row)) << 5) + (c & 1) * 16) = r3[i]; }
    }
    bar();
    {
      u32x4 wof[4];
#pragma unroll
      for (int ks = 0; ks < 4; ++ks) wof[ks] = wfrag_dense(L + 131072, lane, w, ks);
      const int tl = lane & 15, g = lane >> 4;
      const rsrc_t rq = mkrsrc(P.wq, 4u << 20), rkw = mkrsrc(P.wk, 4u << 20);
      const u32 wvo_ = opq((u32)(tid >> 4) * 8192 + (tid & 15) * 16);
#pragma unroll
      for (int c4 = 0; c4 < 4; ++c4) {
        const int row = sp_row(c4, tl >> 2, tl & 3), f = (row & 1) | ((row >> 1) & 14);
        u32 a4[4];
#pragma unroll
        for (int ks = 0; ks < 4; ++ks) a4[ks] = opq(row * 256 + (((4 * ks + g) ^ f) << 4));
        u32x4 fa[2][4];
#pragma unroll
        for (int ks = 0; ks < 4; ++ks) fa[0][ks] = lds16(L + a4[ks]);
#pragma unroll
        for (int i = 0; i < 8; ++i) {
          sched();
          if (i < 7) {
#pragma unroll
            for (int ks = 0; ks < 4; ++ks) fa[(i + 1) & 1][ks] = lds16(L + a4[ks] + ((i + 1) & 3) * 16384 + ((i + 1) >> 2) * 65536);
          }
          {
            const int n = c4 * 8 + i;
            const u32 so = h * 256 + (((n & 15) + 4 * c4w) & 15) * 262144;
            if (n < 16) r0[n] = __builtin_amdgcn_raw_buffer_load_b128(rq, wvo_, so, 0); else r1[n - 16] = __builtin_amdgcn_raw_buffer_load_b128(rkw, wvo_, so, 0);
          }
          sched();
          f32x4 d = {0, 0, 0, 0};
#pragma unroll
          for (int ks = 0; ks < 4; ++ks) d = mfma32(fa[i & 1][ks], wof[ks], d);
          bw[i][2 * c4] = opq(pk2(d[0], d[1])); bw[i][2 * c4 + 1] = opq(pk2(d[2], d[3]));
        }
      }
    }
    bar();
    const ldsp R = L + C_RAW;
    u32 ad[4], ah[4];
    {
      const int ln = opq(lane);
#pragma unroll
      for (int c = 0; c < 4; ++c) {
        const int row = sp_row(c, ln >> 4, (ln >> 2) & 3);
        ad[c] = opq(C_RAW + row * 256 + ((w ^ wswz<1>(row)) << 5) + (ln & 3) * 8); ah[c] = opq(ad[c] + 65536);
      }
    }
    wraw_store<1>(R, opq(tid), r0, 4 * c4w);
    bar();
#pragma unroll
    for (int i = 0; i < 4; ++i) { bq[i] = wfrag_sparse(L + i * 16384, ad); bq[4 + i] = wfrag_sparse(L + i * 16384, ah); sched(); }
    bar();
    wraw_store<1>(R, opq(tid), r1, 4 * c4w);
    bar();
#pragma unroll
    for (int i = 0; i < 4; ++i) { bk[i] = wfrag_sparse(L + i * 16384, ad); bk[4 + i] = wfrag_sparse(L + i * 16384, ah); sched(); }
    bar();
#pragma unroll
    for (int i = 0; i < 7; ++i) if (w == 0) lds32w(L + C_W + lane * 16 + i * 2048, bw[1 + i]);
    const int t8 = tid & 255;
    const u16* mp = P.mem0 + (2 * (t8 & 3)) * DM + (t8 >> 2) * 4;
    const u32x2 m0 = *(const u32x2*)mp, m1 = *(const u32x2*)(mp + DM), m2 = *(const u32x2*)(mp + 256), m3 = *(const u32x2*)(mp + DM + 256);
    mv = (u32x4){m0[0], m0[1], m1[0], m1[1]}; mw = (u32x4){m2[0], m2[1], m3[0], m3[1]};
  }
  const rsrc_t rx = mkrsrc(P.ws, 160u << 20);
  const u32 xo = (u32)(xch - P.ws);
  const u32 kb0 = (u32)OFF_KX + h * 8192, vb0 = (u32)OFF_VW + h * 32768 + c4w * 8192, mb0 = (u32)OFF_KM + h * 2048, wb0 = (u32)OFF_VWM + h * 8192 + c4w * 2048;
  u64 msel; u32 pub, own0, own1; u64 flipm;
  {
    u32 t0 = 0, t1 = 0, t2 = 0, n = 0;
    GAS u32* T = C.line(L_TBL);
    for (;;) {
      t0 = __hip_atomic_load(T + lane, RLX_AGENT); t1 = __hip_atomic_load(T + 64 + lane, RLX_AGENT); t2 = __hip_atomic_load(T + 128 + lane, RLX_AGENT);
      if (__all(t0 != 0 && t1 != 0 && t2 != 0) || C.dead) break;
      __builtin_amdgcn_s_sleep(8);
      if (C.spin(n)) break;
    }
    msel = __ballot(t0 == 2);
    const u64 s0 = __ballot(t1 == 2), s1 = __ballot(t2 == 2);
    const int r = 32 * c4w + 4 * w + (lane >> 4), b = r >> 1;
    pub = opq(b * 16384 + (u32)((s0 >> b) & 1) * 8192 + (r & 1) * 4096 + h * 128 + (lane & 7) * 16);
    flipm = __ballot((((s0 ^ s1) >> b) & 1) != 0);
    own0 = xo + (64 + (id >> 1)) * 16384 + (u32)((s0 >> (id >> 1)) & 1) * 8192 + (id & 1) * 4096;
    own1 = xo + (128 + (id >> 1)) * 16384 + (u32)((s1 >> (id >> 1)) & 1) * 8192 + (id & 1) * 4096;
  }
  const int sidx = (lane & 8) ? 0xEEEE : 0x4444;
  const u32 t16 = tid * 16;
  u32 seenK = 0, seenV = 0;
  if (w >= 4) {
    seenK = C.wait_ge(L_KV + h, 1); seenV = C.wait_ge(L_KV + 32 + h, 1);
    const u32 o = t16 - 4096;
    const u32x4 k0 = ld16o(rx, o, kb0), k1 = ld16o(rx, o, kb0 + 4096);
    const u32x4 v0 = ld16o(rx, o, vb0), v1 = ld16o(rx, o, vb0 + 4096);
    *(LAS u32x4*)(L + C_KX + o) = k0; *(LAS u32x4*)(L + C_KX + 4096 + o) = k1;
    *(LAS u32x4*)(L + C_VX + o) = v0; *(LAS u32x4*)(L + C_VX + 4096 + o) = v1;
  }
  const u32 a_mw = opq((tid >> 3) * 128 + (tid & 3) * 32 + ((tid >> 2) & 1) * 8);
  const u32 l16p = opq(lane * 16);
  const u32 tso = xo + (u32)(msel & 1) * 8192;
  u32 tg2 = 0, nest = 0, tbase = 0, ext2 = EXT0;
  int d1 = PD2;
  bool slipped = false;
  auto put_mem = [&](const u32x4& a, const u32x4& b) __attribute__((always_inline)) {
    *(LAS u32x2*)(L + C_MEM + a_mw) = (u32x2){a[0], a[1]}; *(LAS u32x2*)(L + C_MEM + a_mw + 16) = (u32x2){a[2], a[3]};
    *(LAS u32x2*)(L + C_MEM + 4096 + a_mw) = (u32x2){b[0], b[1]}; *(LAS u32x2*)(L + C_MEM + 4096 + a_mw + 16) = (u32x2){b[2], b[3]};
  };
  if (w < 4) put_mem(mv, mw);
  auto step_loop = [&](auto role_c) __attribute__((always_inline)) {
  constexpr int ROLE = decltype(role_c)::value;
  f32x4 aq = zero4(), ak = zero4();
  for (int s = 0; s < NSEG - 1; ++s) {
    bar();
    u32x4 af[8];
#pragma unroll
    for (int i = 0; i < 3; ++i) af[i] = lds16(L + C_MEM + i * 1024 + l16p);
    sched();
    const u32 kfo = (s & 1) * 12288;
    u32 a_fr = 0, a_kr = 0, aq_ = 0, ak_ = 0;
    u32x4 k1f[4], k2f[4];
#pragma unroll
    for (int i = 0; i < 8; ++i) {
      sched();
      if (i + 3 < 8) af[i + 3] = lds16(L + C_MEM + (i + 3) * 1024 + l16p);
      if (ROLE == 0 && i >= 4) { k1f[i - 4] = lds16(L + C_KX + (i - 4) * 1024 + l16p); k2f[i - 4] = lds16(L + C_KX + 4096 + (i - 4) * 1024 + l16p); }
      sched();
      aq = smf(af[i], bq[i], aq, sidx); ak = smf(af[i], bk[i], ak, sidx);
      pin(aq); pin(ak);
      if (ROLE == 0 && i == 1) { const u32 l16 = opq(l16p); a_fr = opq((l16 >> 8) * 128 + (l16 & 112)); a_kr = opq(a_fr + kfo); }
      if (i == (ROLE == 2 ? 5 : 3)) {
        const u32 l16 = opq(l16p);
        const u32 a_fw = (w & 1) * 8 + ((l16 >> 6) & 3) * 128 + ((l16 >> 8) & 1) * 64 + (l16 >> 9) * 32 + ((l16 >> 4) & 3) * 2;
        aq_ = opq(a_fw + (w >> 1) * 512); ak_ = opq(a_fw + (w >> 1) * 1024 + kfo);
      }
    }
    sched();
    {
      const u32 qp = half_sum_pk(aq);
      *(LAS u16*)(L + C_QF + aq_) = (u16)qp; *(LAS u16*)(L + C_QF + aq_ + 16) = (u16)(qp >> 16);
      const u32 kp = half_sum_pk(ak);
      *(LAS u16*)(L + C_KF + ak_) = (u16)kp; *(LAS u16*)(L + C_KF + ak_ + 16) = (u16)(kp >> 16);
    }
    f32x4 av = zero4();
    sched();
    if (ROLE == 0) __builtin_amdgcn_s_setprio(3);
    bar();
    u32x4 vxf, k0, k1, v0, v1;
    u32 a_p, a_p0, vpk = 0, o = 0;
    u32x8 su[7];
    if (ROLE == 0) {
      u32x4 qf[4], k0f[4];
#pragma unroll
      for (int p = 0; p < 4; ++p) { qf[p] = lds16(L + C_QF + p * 512 + a_fr); k0f[p] = lds16(L + C_KF + p * 1024 + a_kr); }
      sched();
      const u32 l16 = opq(l16p);
      a_p = opq((l16 >> 8) * 128 + (l16 & 112)); a_p0 = opq((l16 >> 2) & ~32u);
      const u32 a_pw = opq(a_p + ((l16 >> 7) & 1) * 8);
      const ldsp LW = L + opq(C_W + l16);
      av = smf(af[0], bw[0], av, sidx);
      pin(av);
      sched();
      f32x4 s0 = {0, 0, 0, 0}, s1 = {0, 0, 0, 0}, s2 = {0, 0, 0, 0};
#pragma unroll
      for (int p = 0; p < 4; ++p) { s1 = mfma32(k1f[p], qf[p], s1); s2 = mfma32(k2f[p], qf[p], s2); s0 = mfma32(k0f[p], qf[p], s0); }
      pin(s0); pin(s1); pin(s2);
      sched();
#pragma unroll
      for (int i = 0; i < 3; ++i) su[i] = lds32(LW + i * 2048);
      sched();
      const float cs = 0.08838834764831845f * 1.4426950408889634f;
      const bool hi8 = (lane & 8) != 0, hi32 = lane >= 32;
      float x[6];
#pragma unroll
      for (int r = 0; r < 4; ++r) x[r] = hi8 ? s2[r] : s1[r];
      x[4] = hi32 ? s0[2] : s0[0]; x[5] = hi32 ? s0[3] : s0[1];
      float mx = vmax(vmax(vmax(x[0], x[1]), vmax(x[2], x[3])), vmax(x[4], x[5]));
      mx = vmax(mx, ror16<8>(mx));
      { float a = mx, b = mx; swap16(a, b); mx = vmax(a, b); }
      { float a = mx, b = mx; swap32(a, b); mx = vmax(a, b); }
      mx *= cs;
#pragma unroll
      for (int r = 0; r < 6; ++r) x[r] = __builtin_amdgcn_exp2f(__builtin_fmaf(x[r], cs, -mx));
      float sum = ((x[0] + x[1]) + (x[2] + x[3])) + (hi8 ? 0.f : x[4] + x[5]);
      sum += ror16<8>(sum);
      sum = xsum_g(sum);
      const float inv = __builtin_amdgcn_rcpf(sum);
      *(LAS u32x2*)(L + C_P + a_pw) = (u32x2){pk2(x[0] * inv, x[1] * inv), pk2(x[2] * inv, x[3] * inv)};
      *(LAS u32*)(L + C_P + 512 + a_p0) = pk2(x[4] * inv, x[5] * inv);
      sched();
#pragma unroll
      for (int i = 3; i < 7; ++i) su[i] = lds32(LW + i * 2048);
      vxf = lds16(L + C_VX + t16);
      sched();
      av = smf(af[1], su[0], av, sidx); av = smf(af[2], su[1], av, sidx); av = smf(af[3], su[2], av, sidx); pin(av);
    } else {
#pragma unroll
      for (int i = 0; i < 4; ++i) av = smf(af[i], bw[i], av, sidx);
      pin(av);
      sched();
      if (ROLE == 2) {
        o = opq(t16) - 4096;
        const u32 kb = kb0 + (s + 1) * 262144, vb = vb0 + (s + 1) * 1048576;
        k0 = af[0]; k1 = af[1]; v0 = af[2]; v1 = af[3];
        if (seenK >= (u32)s + 2 && seenV >= (u32)s + 2) { k0 = ld16o(rx, o, kb); k1 = ld16o(rx, o, kb + 4096); v0 = ld16o(rx, o, vb); v1 = ld16o(rx, o, vb + 4096); }
      }
      vxf = lds16(L + C_VX + t16);
      sched();
#pragma unroll
      for (int i = 4; i < 8; ++i) av = smf(af[i], bw[i], av, sidx);
      const u32 l16 = opq(l16p);
      a_p = opq((l16 >> 8) * 128 + (l16 & 112)); a_p0 = opq((l16 >> 2) & ~32u);
      vpk = opq(half_sum_pk(av));
      sched();
      if (ROLE == 1) {
        if (c4w == 0) {
          const u32 a = C_KF + kfo + (l16 >> 9) * 1024 + (l16 & 511);
          st16o(lds16(L + a), rx, l16, mb0 + s * 65536); st16o(lds16(L + a + 2048), rx, l16, mb0 + s * 65536 + 1024);
        }
        if (s > 0) {
          const u32 so = (s & 1) ? own0 : own1;
          const u32x4 z = sent4();
          st16o(z, rx, l16, so); st16o(z, rx, l16, so + 1024); st16o(z, rx, l16, so + 2048); st16o(z, rx, l16, so + 3072);
          const u32 a = C_VP + (l16 >> 8) * 256 + ((l16 >> 4) & 15) * 4;
          const u32x4 d0 = {*(LAS u32*)(L + a), *(LAS u32*)(L + a + 128), *(LAS u32*)(L + a + 64), *(LAS u32*)(L + a + 192)};
          const u32x4 d1 = {*(LAS u32*)(L + a + 1024), *(LAS u32*)(L + a + 1152), *(LAS u32*)(L + a + 1088), *(LAS u32*)(L + a + 1216)};
          st16o(d0, rx, l16, wb0 + (s - 1) * 262144); st16o(d1, rx, l16, wb0 + (s - 1) * 262144 + 1024);
        }
      }
    }
    const u32 zc = vconst(0u);
    if (ROLE == 0) { sched(); asm volatile("s_waitcnt lgkmcnt(9)\n\ts_barrier" ::: "memory"); sched(); }
    else bar();
    {
      const u32x4 px = lds16(L + C_P + a_p);
      const u32 p0 = *(LAS u32*)(L + C_P + 512 + a_p0);
      sched();
      if (ROLE == 0) {
        av = smf(af[4], su[3], av, sidx); av = smf(af[5], su[4], av, sidx); av = smf(af[6], su[5], av, sidx); av = smf(af[7], su[6], av, sidx);
        vpk = half_sum_pk(av);
      }
      const f32x4 z = {0, 0, 0, 0};
      const u32 pso = xo + ((s & 1) ? 128 * 16384 : 64 * 16384);
      sched();
      const f32x4 on = mfma32(vxf, px, z) + mfma16((u32x2){vpk, zc}, (u32x2){p0, zc}, z);
      st16o(__builtin_bit_cast(u32x4, on), rx, pub, pso);
      asm volatile("s_mov_b64 exec, %1\n\tv_xor_b32 %0, 0x2000, %0\n\ts_mov_b64 exec, -1" : "+v"(pub) : "s"(flipm));
      *(LAS u32*)(L + C_VP + (t16 >> 2)) = vpk;
      sched();
    }
    if (ROLE == 0) __builtin_amdgcn_s_setprio(0);
    aq = zero4(); ak = zero4();
    if (ROLE == 2) {
      if (seenK < (u32)s + 2 || seenV < (u32)s + 2) {
        seenK = C.wait_ge(L_KV + h, s + 2); seenV = C.wait_ge(L_KV + 32 + h, s + 2);
        const u32 kb = kb0 + (s + 1) * 262144, vb = vb0 + (s + 1) * 1048576;
        k0 = ld16o(rx, o, kb); k1 = ld16o(rx, o, kb + 4096); v0 = ld16o(rx, o, vb); v1 = ld16o(rx, o, vb + 4096);
      }
      if (seenK < (u32)s + 6 && seenK < (u32)NSEG) seenK = uni(__hip_atomic_load(C.line(L_KV + h), RLX_AGENT));
      if (seenV < (u32)s + 6 && seenV < (u32)NSEG) seenV = uni(__hip_atomic_load(C.line(L_KV + 32 + h), RLX_AGENT));
      bar();
      *(LAS u32x4*)(L + C_KX + o) = k0; *(LAS u32x4*)(L + C_KX + 4096 + o) = k1;
      *(LAS u32x4*)(L + C_VX + o) = v0; *(LAS u32x4*)(L + C_VX + 4096 + o) = v1;
    } else {
      const u32 mo = xo + (u32)(s + 1) * 16384 + (u32)((msel >> (s + 1)) & 1) * 8192;
      u32 n = 0;
      if (ROLE == 1) {
        const u32 so = (s & 1) ? own1 : own0;
        const u32 l16 = opq(l16p);
        const u32 ao = opq(((l16 >> 4) & 7) * 8 + (l16 >> 9) * 4);
        u32x4 p0, p1, p2, p3;
        for (int d = 0; d < d1; ++d) __builtin_amdgcn_s_sleep(1);
        cbar();
        p0 = ld16o(rx, l16, so); p1 = ld16o(rx, l16, so + 1024); p2 = ld16o(rx, l16, so + 2048); p3 = ld16o(rx, l16, so + 3072);
        if (__builtin_expect(!all_set(__builtin_elementwise_max(__builtin_elementwise_max(p0, p1), __builtin_elementwise_max(p2, p3))), 0)) {
          if (ADAPT && s >= ASTART && d1 < 12) ++d1;
          while (!C.dead) {
            cbar();
            p0 = ld16o(rx, l16, so); p1 = ld16o(rx, l16, so + 1024); p2 = ld16o(rx, l16, so + 2048); p3 = ld16o(rx, l16, so + 3072);
            if (all_set(__builtin_elementwise_max(__builtin_elementwise_max(p0, p1), __builtin_elementwise_max(p2, p3))) || C.spin(n)) break;
          }
        }
        const f32x4 t = (__builtin_bit_cast(f32x4, p0) + __builtin_bit_cast(f32x4, p1)) + (__builtin_bit_cast(f32x4, p2) + __builtin_bit_cast(f32x4, p3));
        float a0 = t[0], a1 = t[1], a2 = t[2], a3 = t[3];
        swap32(a0, a2); swap32(a1, a3);
        float u0 = a0 + a2, u1 = a1 + a3;
        swap16(u0, u1);
        u0 += u1;
        u0 += ror16<8>(u0);
        float b0 = u0, b1 = u0;
        swap16(b0, b1);
        __builtin_amdgcn_raw_buffer_store_b32(nosent(pk2(b0, b1)), rx, ao, mo + id * 64, SC1);
        if (id == TSWG) __builtin_amdgcn_raw_buffer_store_b32(nosent((u32)__builtin_amdgcn_s_memrealtime()), rx, 0, tso + s * 128, SC1);
        if (lane == 0) C.post(L_CH + id, s + 1);
      }
      if (s >= 1) {
        const u32 tsm = uni(*(LAS u32*)(L + C_TS + vconst(w * 4)));
        if (s >= 2 && (s <= 4 || !slipped)) {
          const u32 nn = tsm - tbase;
          nest = s <= 4 ? (s >= 3 && nn > NMAX0 ? NMAX0 : nn) : (nn > nest + 8 ? nest + 2 : (EMA * nest + nn + (EMA + 1) / 2) / (EMA + 1));
        }
        u32 base = tg2;
        slipped = (int)(tsm + DELTA2 - LATE - tg2) > 0;
        if (s < 3 || slipped) base = tsm + DELTA2 + (s < 3 ? 0 : SLIP);
        tbase = base;
        tg2 = base + nest + DELTA2;
      }
      bar();
      n = 0;
      {
        const u32 now = (u32)__builtin_amdgcn_s_memrealtime();
        if (s >= 2) {
          int k = ((int)(tg2 + ext2 - now) * SLNUM) >> 5;
          k = k < PD1MIN ? PD1MIN : (k > 64 ? 64 : k);
          for (int d = 0; d < k; ++d) __builtin_amdgcn_s_sleep(1);
        } else {
          for (int d = 0; d < PD1; ++d) __builtin_amdgcn_s_sleep(1);
        }
      }
      cbar();
      u32 ts = __builtin_amdgcn_raw_buffer_load_b32(rx, 0, tso + s * 128, SC1);
      u32x4 ma = ld16o(rx, t16, mo), mb = ld16o(rx, t16, mo + 4096);
      {
        u32x4 mm = __builtin_elementwise_max(ma, mb); mm[0] = mm[0] > ts ? mm[0] : ts;
        if (__builtin_expect(!all_set(mm), 0)) {
          if (ADAPT && s >= ASTART && ext2 < 16) ext2 += 2;
          while (!C.dead) {
            cbar();
            ts = __builtin_amdgcn_raw_buffer_load_b32(rx, 0, tso + s * 128, SC1);
            ma = ld16o(rx, t16, mo); mb = ld16o(rx, t16, mo + 4096);
            mm = __builtin_elementwise_max(ma, mb); mm[0] = mm[0] > ts ? mm[0] : ts;
            if (all_set(mm) || C.spin(n)) break;
          }
        }
      }
      put_mem(ma, mb);
      *(LAS u32*)(L + C_TS + vconst(w * 4)) = ts;
      sched();
    }
  }
  {
    const int s = NSEG - 1;
    bar();
    if (w < 2) {
      const u32 a = C_VP + (t16 >> 8) * 256 + ((t16 >> 4) & 15) * 4;
      const u32x4 d = {*(LAS u32*)(L + a), *(LAS u32*)(L + a + 128), *(LAS u32*)(L + a + 64), *(LAS u32*)(L + a + 192)};
      st16o(d, rx, t16, wb0 + (s - 1) * 262144);
    }
    const u32 l16 = l16p;
    const ldsp LW = L + C_W + l16;
    f32x4 av = {0, 0, 0, 0};
#pragma unroll
    for (int i = 0; i < 8; ++i) {
      const u32x4 af = lds16(L + C_MEM + i * 1024 + l16);
      ak = smf(af, bk[i], ak, sidx);
      av = smf(af, (ROLE != 0 || i < 1) ? bw[i] : lds32(LW + (i - 1) * 2048), av, sidx);
    }
    const u32 a_fw = (u32)(w >> 1) * 1024 + (w & 1) * 8 + ((l16 >> 6) & 3) * 128 + ((l16 >> 8) & 1) * 64 + (l16 >> 9) * 32 + ((l16 >> 4) & 3) * 2;
    const u32 kp = half_sum_pk(ak), vpk = half_sum_pk(av);
    *(LAS u16*)(L + C_KF + 12288 + a_fw) = (u16)kp; *(LAS u16*)(L + C_KF + 12288 + a_fw + 16) = (u16)(kp >> 16);
    __builtin_amdgcn_raw_buffer_store_b32(vpk, rx, (l16 & 240) + ((l16 >> 8) & 1) * 8 + (l16 >> 9) * 4, wb0 + s * 262144 + w * 256, SC1);
    bar();
    if (c4w == 0 && w >= 6) st16o(lds16(L + C_KF + 12288 + (w - 6) * 2048 + (l16 >> 9) * 1024 + (l16 & 511)), rx, t16 - 6144, mb0 + s * 65536);
    drain();
    bar();
    if (tid == 0) C.post(L_CH + id, NSEG + 1);
  }
  };
  if (w == 0) step_loop(std::integral_constant<int, 0>());
  else if (w == 3) step_loop(std::integral_constant<int, 1>());
  else if (w >= 4) step_loop(std::integral_constant<int, 2>());
  else step_loop(std::integral_constant<int, 3>());
}

__global__ void __launch_bounds__(512, 2) rmt_kernel(Params P) {
  extern __shared__ __attribute__((aligned(16))) unsigned char smem[];
  ldsp L = (ldsp)smem;
  Ctl C; C.c = (GAS u32*)P.ws; C.dead = false; C.t_in = (u32)__builtin_amdgcn_s_memrealtime();
  unsigned char* xch = (unsigned char*)(((uintptr_t)(P.ws + OFF_XCH) + 16383) & ~(uintptr_t)16383);
  u32x4 cpf = {0, 0, 0, 0};
  if (threadIdx.x >= 256) {
    u64 pc; u32 len;
    asm volatile(".Lrmt_code_begin:\n\ts_getpc_b64 %0\n\ts_mov_b32 %1, .Lrmt_code_end-.Lrmt_code_begin" : "=s"(pc), "=s"(len));
    const rsrc_t rc = mkrsrc((const void*)(pc & ~(u64)63), len & ~63u);
    cpf = __builtin_amdgcn_raw_buffer_load_b128(rc, ((blockIdx.x >> 3) & 31) * 4096 + (threadIdx.x & 255) * 16, 0, 0);
  }
  u32x4 pf[8];
#pragma unroll
  for (int i = 0; i < 8; ++i) pf[i] = (u32x4){0, 0, 0, 0};
  if (threadIdx.x >= 256) {
    const u32 xg = (u32)__builtin_amdgcn_s_getreg(20 | (3 << 11)) & 7u, j = (blockIdx.x >> 3) & 31, t8 = threadIdx.x & 255;
    const rsrc_t a = mkrsrc(xg < 6 ? P.wv : P.x, xg < 6 ? 4u << 20 : 2u << 20);
    const u32 vo = xg < 4 ? (16 * j + (t8 >> 7)) * 8192 + xg * 2048 + (t8 & 127) * 16 : (xg < 6 ? 8 * j * 8192 + (xg & 1) * 4096 + t8 * 16 : (xg & 1) * 1048576 + j * 32768 + t8 * 16);
    const u32 st = xg < 4 ? 16384 : (xg < 6 ? 8192 : 4096);
#pragma unroll
    for (int i = 0; i < 8; ++i) pf[i] = __builtin_amdgcn_raw_buffer_load_b128(a, vo, i * st, 0);
  }
  u32x4 pg[16];
#pragma unroll
  for (int i = 0; i < 16; ++i) pg[i] = (u32x4){0, 0, 0, 0};
  if (threadIdx.x >= 64 && threadIdx.x < 192) {
    const u32 xg = (u32)__builtin_amdgcn_s_getreg(20 | (3 << 11)) & 7u, j = (blockIdx.x >> 3) & 31, t8 = threadIdx.x - 64;
    const u32 W = 128 * 16;
    if (xg < 6) {
      const rsrc_t a = mkrsrc(xg < 4 ? P.wo : P.wv, 4u << 20);
      const u32 vo = xg < 4 ? xg * 1048576 + j * 32768 + t8 * 16 : (256 + 8 * j) * 8192 + (xg & 1) * 4096 + t8 * 16;
#pragma unroll
      for (int i = 0; i < 16; ++i) pg[i] = __builtin_amdgcn_raw_buffer_load_b128(a, vo, xg < 4 ? i * W : (i >> 1) * 8192 + (i & 1) * 2048, 0);
    }
  }
  {
    const u64 t_in = __builtin_amdgcn_s_memrealtime();
    GAS u64* tok = (GAS u64*)(P.ws + CTRL_BYTES);
    if (blockIdx.x == 0) {
      const rsrc_t rc = mkrsrc(P.ws, CTRL_BYTES);
      for (u32 o = threadIdx.x * 16; o < CTRL_BYTES; o += 8192) st16s(rc, o, (u32x4){0, 0, 0, 0});
      drain();
      bar();
      if (threadIdx.x == 0) __hip_atomic_store(tok, (u64)__builtin_amdgcn_s_memrealtime(), RLX_AGENT);
    }
    if (threadIdx.x == 0) {
      const u64 lo = t_in - 12000;
      for (u32 n = 0; n < (1u << 20); ++n) {
        const u64 v = __hip_atomic_load(tok, RLX_AGENT);
        if (v - lo <= (u64)__builtin_amdgcn_s_memrealtime() - lo) break;
        __builtin_amdgcn_s_sleep(1);
      }
      __builtin_amdgcn_fence(__ATOMIC_ACQUIRE, "wavefront");
    }
  }
  if (threadIdx.x == 0) {
    const u32 x = (u32)__builtin_amdgcn_s_getreg(20 | (3 << 11)) & 7u;
    u32 t = __hip_atomic_fetch_add(C.line(L_TICKET + x), 1u, RLX_AGENT);
    u32 slot = x * 32 + t;
    if (t >= 32) {
      const u32 o = __hip_atomic_fetch_add(C.line(L_OVF), 1u, RLX_AGENT);
      u32 cnt[8], n = 0;
      for (;;) {
        u32 tot = 0;
        for (int i = 0; i < 8; ++i) { cnt[i] = __hip_atomic_load(C.line(L_TICKET + i), RLX_AGENT); tot += cnt[i]; }
        if (tot >= 256) break;
        __builtin_amdgcn_s_sleep(8);
        if (++n > (1u << 20)) break;
      }
      u32 k = o; slot = 255;
      for (int i = 0; i < 8; ++i) { const u32 fr = cnt[i] < 32 ? 32 - cnt[i] : 0; if (k < fr) { slot = i * 32 + cnt[i] + k; break; } k -= fr; }
    }
    *(LAS u32*)L = slot;
  }
  bar();
  const int slot = uni(*(LAS u32*)L);
  asm volatile("" :: "v"(cpf));
#pragma unroll
  for (int i = 0; i < 16; ++i) asm volatile("" :: "v"(pg[i]));
#pragma unroll
  for (int i = 0; i < 8; ++i) asm volatile("" :: "v"(pf[i]));
  bar();
  const int x = slot >> 5, t = slot & 31;
  if (x < 4) role_chain(P, C, xch, slot);
  else {
    const int i = (x & 1) * 16 + (t >> 1), r = x < 6 ? (t & 1) : 2 + (t & 1);
    if (r == 1 || r == 2) {
      if (r == 1) role_v(P, C, L, i); else role_probe(C, xch, i, (((u32)__builtin_amdgcn_s_getreg(20 | (3 << 11)) >> 2) & 1u) != 0u);
      role_attn(P, C, L, i, r == 1);
    } else {
      if (r == 0) role_k(P, C, L, i);
      else {
        role_router(P, C, L, i);
        bar();
        role_ffn(P, C, L, i);
      }
      role_out(P, C, L, i >> 1, i & 1, r == 0);
    }
  }
  asm volatile(".Lrmt_code_end:");
}

extern "C" void kernel_launch(void* const* d_in, const int* in_sizes, int n_in,
                              void* d_out, int out_size, void* d_ws, size_t ws_size,
                              hipStream_t stream) {
  Params p;
  p.x = (const u16*)d_in[0]; p.mem0 = (const u16*)d_in[1]; p.wq = (const u16*)d_in[2]; p.wk = (const u16*)d_in[3];
  p.wv = (const u16*)d_in[4]; p.wo = (const u16*)d_in[5]; p.wr = (const u16*)d_in[8]; p.wg = (const u16*)d_in[9];
  p.wu = (const u16*)d_in[10]; p.wd = (const u16*)d_in[11];
  p.out = (u16*)d_out; p.ws = (unsigned char*)d_ws;
  hipFuncSetAttribute((const void*)rmt_kernel, hipFuncAttributeMaxDynamicSharedMemorySize, LDS_BYTES);
  hipLaunchKernelGGL(rmt_kernel, dim3(256), dim3(512), LDS_BYTES, stream, p);
}
```
